```python
import torch

def _softmax(x, axis=-1):
    return torch.softmax(x, dim=axis)
def _gelu(x):
    return torch.nn.functional.gelu(x, approximate="tanh")
def _silu(x):
    return torch.nn.functional.silu(x)
def _sigmoid(x):
    return torch.sigmoid(x)
def _layernorm(x, axis=-1, eps=1e-5):
    m = x.mean(dim=axis, keepdim=True)
    v = x.var(dim=axis, keepdim=True, unbiased=False)
    return (x - m) / torch.sqrt(v + eps)
def _scatter_add(tgt, idx, src, axis=0):
    return tgt.clone().index_add_(axis, idx, src)
def _segment_sum(src, seg, n):
    out = torch.zeros((n,) + src.shape[1:], dtype=src.dtype)
    return out.index_add_(0, seg, src)
def _pad(x, pw):
    flat = tuple(v for pair in reversed(pw) for v in pair)
    return torch.nn.functional.pad(x, flat)

kernel_name = 'recurrent_scan__spectral_fnet__mixer_channel_mlp__prompt'

def setup_inputs():

    _w = (lambda a: (lambda t: t.to(torch.bfloat16) if t.is_floating_point() else t)(torch.as_tensor(a)))
    def _A_setup():
        B, S, D = 1, 2048, 576
        ns = torch.arange(S)[:, None] * torch.arange(S)[None, :]
        nd = torch.arange(D)[:, None] * torch.arange(D)[None, :]
        Ws_re = torch.cos(-2.0 * torch.pi * ns / S).to(float)
        Ws_im = torch.sin(-2.0 * torch.pi * ns / S).to(float)
        Wd_re = torch.cos(-2.0 * torch.pi * nd / D).to(float)
        Wd_im = torch.sin(-2.0 * torch.pi * nd / D).to(float)
        return {
            "x": _w((lambda *s: torch.randn(*s, dtype=torch.bfloat16) * 0.05)(B, S, D)),
            "dft_seq_re": _w(Ws_re),
            "dft_seq_im": _w(Ws_im),
            "dft_hid_re": _w(Wd_re),
            "dft_hid_im": _w(Wd_im),
        }
    _A_in = _A_setup()

    def _B_setup():
        B, S, D, F = 1, 2048, 576, 4608
        return {
            "x": _w((lambda *s: torch.randn(*s, dtype=torch.bfloat16) * 0.05)(B, S, D)),
            "w1": _w((lambda *s: torch.randn(*s, dtype=torch.bfloat16) * 0.8)(D, F) * (D ** -0.5)),
            "b1": _w((lambda *s: torch.randn(*s, dtype=torch.bfloat16) * 0.8)(F)),
            "w2": _w((lambda *s: torch.randn(*s, dtype=torch.bfloat16) * 0.05)(F, D) * (F ** -0.5)),
            "b2": _w((lambda *s: torch.randn(*s, dtype=torch.bfloat16) * 0.05)(D)),
        }
    _B_in = _B_setup()

    return {
        "A_x": _A_in["x"],
        "A_dft_seq_re": _A_in["dft_seq_re"],
        "A_dft_seq_im": _A_in["dft_seq_im"],
        "A_dft_hid_re": _A_in["dft_hid_re"],
        "A_dft_hid_im": _A_in["dft_hid_im"],
        "B_x": _B_in["x"],
        "B_w1": _B_in["w1"],
        "B_b1": _B_in["b1"],
        "B_w2": _B_in["w2"],
        "B_b2": _B_in["b2"],
    }

def reference(A_x, A_dft_seq_re, A_dft_seq_im, A_dft_hid_re, A_dft_hid_im, B_x, B_w1, B_b1, B_w2, B_b2):
    def _A_ref(x, dft_seq_re, dft_seq_im, dft_hid_re, dft_hid_im):
        a = torch.einsum("bsd,ed->bse", x, dft_hid_re)
        b = torch.einsum("bsd,ed->bse", x, dft_hid_im)
        y = torch.einsum("ts,bse->bte", dft_seq_re, a) - torch.einsum("ts,bse->bte", dft_seq_im, b)
        return y

    def _B_ref(x, w1, b1, w2, b2):
        y = _layernorm(x, axis=-1)
        h = _gelu(torch.einsum("bsd,df->bsf", y, w1) + b1)
        return torch.einsum("bsf,fd->bsd", h, w2) + b2

    x = A_x
    xn = _layernorm(x, axis=-1)
    s = _A_ref(xn, A_dft_seq_re, A_dft_seq_im, A_dft_hid_re, A_dft_hid_im)
    g = _silu(_B_ref(xn, B_w1, B_b1, B_w2, B_b2))
    return x + s * g
```

```cpp
#include <hip/hip_runtime.h>
#include <hip/hip_bf16.h>
typedef unsigned short u16;
__device__ __forceinline__ float bf2f(u16 v){ return __uint_as_float(((unsigned)v)<<16); }
__device__ __forceinline__ u16 f2bf(float f){ unsigned u=__float_as_uint(f); u += 0x7fffu + ((u>>16)&1u); return (u16)(u>>16); }

#define S 2048
#define D 576
#define F 4608

__global__ void ln_kernel(const u16* x, u16* xn, u16* y){
  int row = blockIdx.x; int l = threadIdx.x;
  float v[9]; float s=0;
  for(int i=0;i<9;i++){ v[i]=bf2f(x[row*D + l + 64*i]); s+=v[i]; }
  for(int o=32;o;o>>=1) s+=__shfl_xor(s,o);
  float m=s/D; float q=0;
  for(int i=0;i<9;i++){ float d=v[i]-m; q+=d*d; }
  for(int o=32;o;o>>=1) q+=__shfl_xor(q,o);
  float r=rsqrtf(q/D+1e-5f);
  float w[9]; s=0;
  for(int i=0;i<9;i++){ u16 b=f2bf((v[i]-m)*r); xn[row*D+l+64*i]=b; w[i]=bf2f(b); s+=w[i]; }
  for(int o=32;o;o>>=1) s+=__shfl_xor(s,o);
  m=s/D; q=0;
  for(int i=0;i<9;i++){ float d=w[i]-m; q+=d*d; }
  for(int o=32;o;o>>=1) q+=__shfl_xor(q,o);
  r=rsqrtf(q/D+1e-5f);
  for(int i=0;i<9;i++){ y[row*D+l+64*i]=f2bf((w[i]-m)*r); }
}
__global__ void ab_kernel(const u16* xn, const u16* wre, const u16* wim, u16* ab){
  int e = blockIdx.x*64+threadIdx.x; int s = blockIdx.y;
  const u16* w = e<D ? wre + e*D : wim + (e-D)*D;
  float acc=0;
  for(int d=0;d<D;d++) acc += bf2f(xn[s*D+d])*bf2f(w[d]);
  if(e<D) ab[s*D+e]=f2bf(acc); else ab[(S+s)*D+(e-D)]=f2bf(-acc);
}
__global__ void h_kernel(const u16* y, const u16* w1, const u16* b1, u16* h){
  int f = blockIdx.x*64+threadIdx.x; int s = blockIdx.y;
  float acc=0;
  for(int d=0;d<D;d++) acc += bf2f(y[s*D+d])*bf2f(w1[d*F+f]);
  float u = acc + bf2f(b1[f]);
  float g = 0.5f*u*(1.f+tanhf(0.7978845608f*(u+0.044715f*u*u*u)));
  h[s*F+f]=f2bf(g);
}
__global__ void out_kernel(const u16* x, const u16* cr, const u16* ci, const u16* ab, const u16* h, const u16* w2, const u16* b2, u16* out){
  int e = blockIdx.x*64+threadIdx.x; int t = blockIdx.y;
  float s1=0, m=0;
  for(int k=0;k<S;k++) s1 += bf2f(cr[t*S+k])*bf2f(ab[k*D+e]);
  for(int k=0;k<S;k++) s1 += bf2f(ci[t*S+k])*bf2f(ab[(S+k)*D+e]);
  for(int k=0;k<F;k++) m += bf2f(h[t*F+k])*bf2f(w2[k*D+e]);
  m += bf2f(b2[e]);
  float g = m/(1.f+__expf(-m));
  out[t*D+e]=f2bf(bf2f(x[t*D+e]) + s1*g);
}

extern "C" void kernel_launch(void* const* d_in, const int* in_sizes, int n_in,
                              void* d_out, int out_size, void* d_ws, size_t ws_size,
                              hipStream_t stream) {
  const u16* x=(const u16*)d_in[0]; const u16* cr=(const u16*)d_in[1]; const u16* ci=(const u16*)d_in[2];
  const u16* wre=(const u16*)d_in[3]; const u16* wim=(const u16*)d_in[4];
  const u16* w1=(const u16*)d_in[6]; const u16* b1=(const u16*)d_in[7]; const u16* w2=(const u16*)d_in[8]; const u16* b2=(const u16*)d_in[9];
  char* ws=(char*)d_ws;
  u16* xn=(u16*)ws; u16* y=(u16*)(ws+4*1024*1024); u16* ab=(u16*)(ws+8*1024*1024); u16* h=(u16*)(ws+16*1024*1024);
  ln_kernel<<<S,64,0,stream>>>(x,xn,y);
  ab_kernel<<<dim3(18,S),64,0,stream>>>(xn,wre,wim,ab);
  h_kernel<<<dim3(72,S),64,0,stream>>>(y,w1,b1,h);
  out_kernel<<<dim3(9,S),64,0,stream>>>(x,cr,ci,ab,h,w2,b2,(u16*)d_out);
}
```

```cpp
#include <hip/hip_runtime.h>
#include <hip/hip_bf16.h>
typedef unsigned short u16;
__device__ __forceinline__ float bf2f(u16 v){ return __uint_as_float(((unsigned)v)<<16); }
__device__ __forceinline__ u16 f2bf(float f){ unsigned u=__float_as_uint(f); u += 0x7fffu + ((u>>16)&1u); return (u16)(u>>16); }

#define S 2048
#define D 576
#define F 4608

__global__ void ln_kernel(const u16* x, u16* xn, u16* y){
  int row = blockIdx.x; int l = threadIdx.x;
  float v[9]; float s=0;
  for(int i=0;i<9;i++){ v[i]=bf2f(x[row*D + l + 64*i]); s+=v[i]; }
  for(int o=32;o;o>>=1) s+=__shfl_xor(s,o);
  float m=s/D; float q=0;
  for(int i=0;i<9;i++){ float d=v[i]-m; q+=d*d; }
  for(int o=32;o;o>>=1) q+=__shfl_xor(q,o);
  float r=rsqrtf(q/D+1e-5f);
  float w[9]; s=0;
  for(int i=0;i<9;i++){ u16 b=f2bf((v[i]-m)*r); xn[row*D+l+64*i]=b; w[i]=bf2f(b); s+=w[i]; }
  for(int o=32;o;o>>=1) s+=__shfl_xor(s,o);
  m=s/D; q=0;
  for(int i=0;i<9;i++){ float d=w[i]-m; q+=d*d; }
  for(int o=32;o;o>>=1) q+=__shfl_xor(q,o);
  r=rsqrtf(q/D+1e-5f);
  for(int i=0;i<9;i++){ y[row*D+l+64*i]=f2bf((w[i]-m)*r); }
}
__global__ void ab_kernel(const u16* xn, const u16* wre, const u16* wim, u16* ab){
  int e = blockIdx.x*64+threadIdx.x; int s = blockIdx.y;
  const u16* w = e<D ? wre + e*D : wim + (e-D)*D;
  float acc=0;
  for(int d=0;d<D;d++) acc += bf2f(xn[s*D+d])*bf2f(w[d]);
  if(e<D) ab[s*D+e]=f2bf(acc); else ab[(S+s)*D+(e-D)]=f2bf(-acc);
}
__global__ void h_kernel(const u16* y, const u16* w1, const u16* b1, u16* h){
  int f = blockIdx.x*64+threadIdx.x; int s = blockIdx.y;
  float acc=0;
  for(int d=0;d<D;d++) acc += bf2f(y[s*D+d])*bf2f(w1[d*F+f]);
  float u = acc + bf2f(b1[f]);
  float g = 0.5f*u*(1.f+tanhf(0.7978845608f*(u+0.044715f*u*u*u)));
  h[s*F+f]=f2bf(g);
}
__global__ void out_kernel(const u16* x, const u16* cr, const u16* ci, const u16* ab, const u16* h, const u16* w2, const u16* b2, u16* out){
  int e = blockIdx.x*64+threadIdx.x; int t = blockIdx.y;
  float s1=0, m=0;
  for(int k=0;k<S;k++) s1 += bf2f(cr[t*S+k])*bf2f(ab[k*D+e]);
  for(int k=0;k<S;k++) s1 += bf2f(ci[t*S+k])*bf2f(ab[(S+k)*D+e]);
  for(int k=0;k<F;k++) m += bf2f(h[t*F+k])*bf2f(w2[k*D+e]);
  m += bf2f(b2[e]);
  float g = m/(1.f+__expf(-m));
  out[t*D+e]=f2bf(bf2f(x[t*D+e]) + s1*g);
}

#define PROBE_KB 16

typedef unsigned u32x4 __attribute__((ext_vector_type(4)));
template<int MODE>
__global__ __launch_bounds__(512) void probe_kernel(char* base, unsigned* sink){
  unsigned long long t0 = __builtin_amdgcn_s_memrealtime();
  auto rsrc = __builtin_amdgcn_make_buffer_rsrc(base, 0, 0x7fffffff, 0x00020000);
  int bid = blockIdx.x; unsigned a=0;
  constexpr int KB = PROBE_KB;
  if(MODE<2){
    u32x4 v[KB/8];
    #pragma unroll
    for(int i=0;i<KB/8;i++){ unsigned off=(unsigned)bid*KB*1024 + i*8192 + threadIdx.x*16; v[i]=__builtin_amdgcn_raw_buffer_load_b128(rsrc,off,0,MODE==1?2:0); }
    #pragma unroll
    for(int i=0;i<KB/8;i++) a+=v[i].x^v[i].y^v[i].z^v[i].w;
  } else {
    u32x4 v={1u,2u,3u,4u};
    #pragma unroll
    for(int i=0;i<KB/8;i++){ unsigned off=(unsigned)bid*KB*1024 + i*8192 + threadIdx.x*16; __builtin_amdgcn_raw_buffer_store_b128(v,rsrc,off,0,16); }
    asm volatile("s_waitcnt vmcnt(0)":::"memory");
  }
  if(a==0x1234567) *sink=a;
  __syncthreads();
  unsigned long long t1 = __builtin_amdgcn_s_memrealtime();
  unsigned long long dt = t1-t0; if(dt>5000) dt=5000;
  while(__builtin_amdgcn_s_memrealtime()-t1 < dt*200) __builtin_amdgcn_s_sleep(10);
}

extern "C" void kernel_launch(void* const* d_in, const int* in_sizes, int n_in,
                              void* d_out, int out_size, void* d_ws, size_t ws_size,
                              hipStream_t stream) {
  const u16* x=(const u16*)d_in[0]; const u16* cr=(const u16*)d_in[1]; const u16* ci=(const u16*)d_in[2];
  const u16* wre=(const u16*)d_in[3]; const u16* wim=(const u16*)d_in[4];
  const u16* w1=(const u16*)d_in[6]; const u16* b1=(const u16*)d_in[7]; const u16* w2=(const u16*)d_in[8]; const u16* b2=(const u16*)d_in[9];
  char* ws=(char*)d_ws;
  u16* xn=(u16*)ws; u16* y=(u16*)(ws+4*1024*1024); u16* ab=(u16*)(ws+8*1024*1024); u16* h=(u16*)(ws+16*1024*1024);
  probe_kernel<0><<<256,512,0,stream>>>((char*)(d_in[8]),(unsigned*)(ws+(200<<20)));
  ln_kernel<<<S,64,0,stream>>>(x,xn,y);
  ab_kernel<<<dim3(18,S),64,0,stream>>>(xn,wre,wim,ab);
  h_kernel<<<dim3(72,S),64,0,stream>>>(y,w1,b1,h);
  out_kernel<<<dim3(9,S),64,0,stream>>>(x,cr,ci,ab,h,w2,b2,(u16*)d_out);
}
```

```cpp
#include <hip/hip_runtime.h>
#include <hip/hip_bf16.h>
typedef unsigned short u16;
typedef short bf16x8 __attribute__((ext_vector_type(8)));
typedef short s16x4 __attribute__((ext_vector_type(4)));
typedef float f32x4 __attribute__((ext_vector_type(4)));
typedef float f32x2 __attribute__((ext_vector_type(2)));
typedef float f32x16 __attribute__((ext_vector_type(16)));
typedef unsigned u32x4 __attribute__((ext_vector_type(4)));
typedef unsigned u32x2 __attribute__((ext_vector_type(2)));
typedef __bf16 bf2 __attribute__((ext_vector_type(2)));
#define LAS __attribute__((address_space(3)))
typedef LAS s16x4* lds_s16x4;
typedef LAS void* lds_vp;
typedef __amdgpu_buffer_rsrc_t rsrc_t;
#define DI __device__ __forceinline__
DI float bf2f(u16 v){ return __uint_as_float(((unsigned)v)<<16); }
DI u16 f2bf(float f){ unsigned u=__float_as_uint(f); u += 0x7fffu + ((u>>16)&1u); return (u16)(u>>16); }
DI unsigned pk_bf16(float lo, float hi){ bf2 v; v[0]=(__bf16)lo; v[1]=(__bf16)hi; return __builtin_bit_cast(unsigned,v); }
DI float opq(float x){ asm("" : "+v"(x)); return x; }
DI rsrc_t mk_rsrc(const void* p){ return __builtin_amdgcn_make_buffer_rsrc((void*)p, 0, 0x7fffffff, 0x00020000); }
#define ROT 0
#define PRIO 1
#define PFD 3
constexpr int SEQ=2048, DM=576, FF=4608;

#define STAMP(K,i)
#define SB() __builtin_amdgcn_sched_barrier(0)
#define BARRIER() do{ SB(); asm volatile("":::"memory"); __builtin_amdgcn_s_barrier(); asm volatile("":::"memory"); SB(); }while(0)
#define WAITV(n) asm volatile("s_waitcnt vmcnt(%0)"::"n"(n):"memory")
#define WAITL0() asm volatile("s_waitcnt lgkmcnt(0)":::"memory")

DI void dma16(rsrc_t r, unsigned lds_addr, unsigned voff, unsigned soff){
  asm volatile("s_mov_b32 m0, %0\n\ts_nop 4\n\tbuffer_load_dwordx4 %1, %2, %3 offen lds"
               :: "s"(lds_addr), "v"(voff), "s"(r), "s"(soff) : "memory");
}

template<int LOFF, int SOFF>
DI void dma16i(rsrc_t r, unsigned lds_base, unsigned voff){
  unsigned tmp;
  asm volatile("s_add_u32 m0, %1, %2\n\ts_mov_b32 %0, %3\n\ts_nop 3\n\tbuffer_load_dwordx4 %4, %5, %0 offen lds"
               : "=&s"(tmp) : "s"(lds_base), "i"(LOFF), "i"(SOFF), "v"(voff), "s"(r) : "memory", "scc");
}
template<int N> struct IC { static constexpr int v = N; };
constexpr int K1_U = 73728;
constexpr int K1_STAT = 2*K1_U;
constexpr int K1_B1   = K1_STAT + 2048;
constexpr int K1_CSP  = K1_B1 + 1536;
constexpr int K1_LDS  = K1_CSP + 2048;

template<int LOFF>
DI void dma16f(rsrc_t r, unsigned lds_base, unsigned voff, unsigned soff){
  asm volatile("s_add_u32 m0, %0, %1\n\ts_nop 3\n\tbuffer_load_dwordx4 %2, %3, %4 offen lds"
               :: "s"(lds_base), "i"(LOFF), "v"(voff), "s"(r), "s"(soff) : "memory", "scc");
}

template<bool AB, int fb>
DI void k1_body(char* smem, const u16* x, const u16* wre, const u16* wim, const u16* w1, const u16* b1, const u16* w2,
                char* himg, char* abimg, char* w2img, int mb, int nb, int L, int wid, int lane, unsigned long long* stamps){
  const unsigned lbase = (unsigned)(size_t)(LAS char*)smem;
  const int w4 = wid&3;
  const int r = lane&31, hh = lane>>5;
  constexpr int WROW = AB ? 1152 : FF*2;
  constexpr int WKT  = AB ? 128 : 64*FF*2;
  const rsrc_t rX = mk_rsrc(x + (size_t)mb*128*DM);
  const unsigned voR0 = (lane>>3)*1152 + (((lane&7) ^ (lane>>4))*16);
  const unsigned voW0 = AB ? voR0 : (unsigned)((lane>>3)*(FF*2) + (((lane&7) ^ (((lane>>4)&1)<<2))*16));
  auto wrsrc = [&](int u)->rsrc_t {
    const int uc = u<=5 ? u : 5; const u16* p;
    if constexpr (AB){ int ec = (nb-12)*384 + uc*64; p = ec>=576 ? wim + (size_t)(ec-576)*DM : wre + (size_t)ec*DM; }
    else p = w1 + nb*384 + uc*64;
    return __builtin_amdgcn_make_buffer_rsrc((void*)p, 0, u<=5 ? 0x7fffffff : 0, 0x00020000);
  };
  if constexpr (!AB && fb==0){ if(wid<3){
    const rsrc_t rb = mk_rsrc(b1 + nb*384 + wid*128);
    asm volatile("s_mov_b32 m0, %0\n\ts_nop 4\n\tbuffer_load_dword %1, %2, 0 offen lds" :: "s"(lbase + K1_B1 + wid*256), "v"(lane*4), "s"(rb) : "memory"); } }
  const unsigned voXa = voR0 + wid*16*1152, voXb = (voR0^64) + (wid*16+8)*1152;
  const unsigned voS = (AB ? (voW0 ^ ((wid&1)<<6)) : voW0) + wid*8*WROW;
  const unsigned lbX = lbase + wid*2048, lbS = lbase + wid*1024;
  unsigned kto[9];
  { const int rho = ROT ? (mb + 3*nb + (nb/3)) % 9 : 0;
    #pragma unroll
    for(int i=0;i<9;i++){ int t = i + rho; if(t>=9) t -= 9; kto[i] = t; } }
  #define DMAX(kt) do{ dma16f<((3*(kt))%18)*8192>(rX, lbX, voXa, kto[kt]*128); dma16f<((3*(kt))%18)*8192+1024>(rX, lbX, voXb, kto[kt]*128); }while(0)
  #define DMAW(rw,slot,kt) dma16f<(slot)*8192>(rw, lbS, voS, kto[kt]*WKT)
  const rsrc_t rw0 = wrsrc(0), rw1 = wrsrc(1), rw2 = wrsrc(2);
  DMAX(0); DMAW(rw0,2,0); DMAX(1); DMAW(rw0,5,1); DMAX(2); DMAW(rw0,8,2);

  unsigned xoff[4];
  unsigned woff[4];
  #pragma unroll
  for(int sp=0;sp<4;sp++){
    int s = sp ^ w4;
    int row = 32*w4 + r;
    xoff[sp] = lbase + row*128 + (((2*s+hh) ^ ((row>>1)&7))<<4);
    if constexpr (AB){ int wr = 32*fb + r; woff[sp] = wr*128 + (((2*s+hh) ^ ((wr>>1)&7))<<4); }
    else { int q=(lane&15)>>2, p=lane&3, blk=(lane>>4)&1; int wr = 16*s + 8*hh + q;
           woff[sp] = wr*128 + ((64*fb + 32*blk + 8*p) ^ (((q>>1)&1)<<6)); }
    woff[sp] += lbase;
  }
  auto wadv = [&](int d){
    #pragma unroll
    for(int sp=0;sp<4;sp++){ woff[sp] += d; asm volatile("" : "+v"(woff[sp])); } };
  auto ldW = [&](int off, int sp)->bf16x8 {
    LAS char* p = (LAS char*)(woff[sp]) + off;
    if constexpr (AB){ return *(const LAS bf16x8*)p; }
    else { s16x4 lo = __builtin_amdgcn_ds_read_tr16_b64_v4i16((lds_s16x4)p);
           s16x4 hi = __builtin_amdgcn_ds_read_tr16_b64_v4i16((lds_s16x4)(p + 512));
           return __builtin_shufflevector(lo,hi,0,1,2,3,4,5,6,7); }
  };
  const short selv = ((lane&15) == ((lane>>4)&1)) ? (short)0x3f80 : (short)0;
  const bf16x8 sel = {selv,selv,selv,selv,selv,selv,selv,selv};

  bf16x8 X[36];
  f32x16 P, Q;
  f32x4 cs = {0.f,0.f,0.f,0.f}, sx = {0.f,0.f,0.f,0.f};
  #pragma unroll
  for(int i=0;i<16;i++){ P[i]=0.f; Q[i]=0.f; }
  constexpr int PD = PFD;
  bf16x8 pw[PD+1];
  STAMP(0,1);
  #define TW(i)
  TW(0);
  if constexpr (fb==1) __builtin_amdgcn_s_setprio(1);
  bf16x8 wf[2][4];
  auto smma = [&](int kt, int h0){
    #pragma unroll
    for(int sp=0;sp<4;sp++){
      P = __builtin_amdgcn_mfma_f32_32x32x16_bf16(wf[kt&1][sp], X[kt*4+sp], P, 0,0,0);
      if(sp==0) cs = __builtin_amdgcn_mfma_f32_16x16x32_bf16(sel, wf[kt&1][0], cs, 0,0,0);
      if((sp>>1)==h0){ Q = __builtin_amdgcn_mfma_f32_32x32x16_bf16(X[kt*4+sp], X[kt*4+sp], Q, 0,0,0);
                       sx = __builtin_amdgcn_mfma_f32_16x16x32_bf16(sel, X[kt*4+sp], sx, 0,0,0); }
    }
    asm volatile("" : "+v"(Q));
    asm volatile("" : "+v"(P), "+v"(sx), "+v"(cs));
  };
  auto sstep = [&](auto KT){
    constexpr int kt = decltype(KT)::v;
    if(kt==3||kt==4) TW(10+3*(kt-3));
    WAITV(6);
    WAITL0();
    if(kt==3||kt==4) TW(11+3*(kt-3));
    BARRIER();
    TW(1+kt);
    #pragma unroll
    for(int sp=0;sp<4;sp++){ X[kt*4+sp] = *(const LAS bf16x8*)((LAS char*)(xoff[sp]) + ((kt&1) ? 24576 : 0)); wf[kt&1][sp] = ldW(16384,sp); }
    SB();
    auto dmas = [&](){
      if constexpr (kt<=5){ DMAX(kt+3); DMAW(rw0,(3*(kt+3)+2)%18,kt+3); }
      else { DMAW(rw1,9+3*(kt-6),3*(kt-6)); DMAW(rw1,10+3*(kt-6),3*(kt-6)+1); DMAW(rw1,11+3*(kt-6),3*(kt-6)+2); }
    };
    if constexpr (kt==0) dmas();
    else if constexpr (fb==0){ dmas(); SB(); smma(kt-1,0); }
    else     { smma(kt-1,1); SB(); dmas(); }
    wadv(kt==5 ? -5*24576 : 24576);
    if(kt&1){
      #pragma unroll
      for(int sp=0;sp<4;sp++){ xoff[sp] += (kt==5 ? -4*24576 : 2*24576); asm volatile("" : "+v"(xoff[sp])); } }
  };
  sstep(IC<0>{}); sstep(IC<1>{}); sstep(IC<2>{}); sstep(IC<3>{}); sstep(IC<4>{}); sstep(IC<5>{}); sstep(IC<6>{}); sstep(IC<7>{}); sstep(IC<8>{});
  smma(8,fb);
  if constexpr (fb==1) __builtin_amdgcn_s_setprio(0);
  auto put_cs = [&](int par){
    if(lane<16){ float* d = (float*)(smem + K1_CSP + par*1024 + wid*128 + lane*4); d[0] = cs[0]; d[16] = cs[1]; }
  };
  put_cs(0);
  if(lane<16){ float* d = (float*)(smem + K1_STAT + fb*512 + (32*w4+lane)*4); d[0] = sx[0]; d[16] = sx[1]; }
  { const int dr = (r&3) + 4*(r>>3); float dg = Q[0];
    #pragma unroll
    for(int i=1;i<16;i++) dg = (dr==i) ? Q[i] : dg;
    if(hh == ((r>>2)&1)) *(float*)(smem + K1_STAT + 1024 + fb*512 + (32*w4+r)*4) = dg; }
  WAITV(0); WAITL0();
  BARRIER();
  STAMP(0,2);
  DMAW(rw2,0,0); DMAW(rw2,1,1); DMAW(rw2,2,2); DMAW(rw2,3,3); DMAW(rw2,4,4);
  #pragma unroll
  for(int i=0;i<PD;i++) pw[i] = ldW(0,i);
  float rr, irr, K0, K1c; bf16x8 Bext;
  { const float* sp_ = (const float*)(smem + K1_STAT + (32*w4+r)*4); const float s1 = sp_[0] + sp_[128], s2 = sp_[256] + sp_[384];
    float mu = s1*(1.f/DM); float var = fmaxf(s2*(1.f/DM) - mu*mu, 0.f);
    float sd = sqrtf(var + 1e-5f);
    irr = sd;
    if constexpr (!AB){ float v2 = var/(var + 1e-5f); irr = sd*sqrtf(v2 + 1e-5f); }
    rr = 1.f/irr;
    K0 = -2.30220820f*rr; K1c = -0.10294324f*rr*rr*rr;
    unsigned i0 = pk_bf16(irr,irr) & 0xffff0000u; unsigned m0 = pk_bf16(-mu,-mu) & 0xffff0000u;
    unsigned wi = pk_bf16(irr, irr - __uint_as_float(i0)), wm = pk_bf16(-mu, -mu - __uint_as_float(m0));
    u32x4 bx = {wi, wm, wm, 0u};
    if(hh) bx = (u32x4){0u,0u,0u,0u};
    Bext = __builtin_bit_cast(bf16x8, bx); }
  const rsrc_t rH = mk_rsrc(AB ? abimg : himg);

  float cp4[4]; unsigned bw = 0; float tz, to[2]; u32x2 pk[2];
  unsigned obase; float a_r = rr; int ce0=0, ri=0;
  auto epi_load = [&](int u){
    if constexpr (AB){ int ec = (nb-12)*384 + u*64; ri = ec>=576; ce0 = ((ec - ri*576)>>3) + 4*fb; a_r = ri ? -rr : rr; obase = hh*512 + ((r ^ (4*hh))*16); }
    else { obase = (unsigned)(((mb*72 + nb*6 + u)*4 + w4)*4096 + fb*2048) + lane*16;
           bw = *(const u16*)(smem + K1_B1 + u*128 + fb*64 + r*2); }
    const float* cp = (const float*)(smem + K1_CSP + (u&1)*1024 + fb*512);
    cp4[0] = cp[r]; cp4[1] = cp[32+r]; cp4[2] = cp[64+r]; cp4[3] = cp[96+r];
  };
  auto epi_fix = [&](f32x16& O){
    float c = opq(opq(cp4[0]+cp4[1]) + opq(cp4[2]+cp4[3]));
    unsigned c0 = pk_bf16(c,c); float c1 = c - __uint_as_float(c0 & 0xffff0000u);
    u32x4 ax = {bw*0x10001u, c0, pk_bf16(c1,c1), 0u};
    O = __builtin_amdgcn_mfma_f32_32x32x16_bf16(__builtin_bit_cast(bf16x8,ax), Bext, O, 0,0,0);
  };
  auto epi_half = [&](const f32x16& O, int e, int half){
    if(half==0){
      if constexpr (!AB){ float v = O[e]; tz = opq(__builtin_amdgcn_exp2f(opq(v*opq(fmaf(opq(v*v), K1c, K0))))); }
    } else {
      if constexpr (AB) to[e&1] = opq(a_r*O[e]);
      else to[e&1] = opq(O[e]*opq(__builtin_amdgcn_rcpf(opq(fmaf(tz, irr, irr)))));
      if(e&1) pk[(e>>2)&1][(e>>1)&1] = pk_bf16(to[0],to[1]);
      if((e&7)==7){
        auto s0 = __builtin_amdgcn_permlane32_swap(pk[0][0], pk[1][0], false, false);
        auto s1 = __builtin_amdgcn_permlane32_swap(pk[0][1], pk[1][1], false, false);
        u32x4 v = {s0[0], s1[0], s0[1], s1[1]};
        unsigned so;
        if constexpr (AB){ int ce = ce0 + (e>>3)*2; int nt = ce/18, cin = ce - nt*18; so = (unsigned)((((ri*4+nt)*64 + mb*4 + w4)*18 + cin)*512); }
        else so = (e>>3)*1024;
        __builtin_amdgcn_raw_buffer_store_b128(v, rH, obase + so, 0, 16);
      }
    }
  };
  #define TS(i)
  rsrc_t rwa = rw2, rwb = wrsrc(3);
  auto interval = [&](f32x16& N, f32x16& O, int u, auto ODDC){
    constexpr int ODD = decltype(ODDC)::v;
    TS(0);
    epi_load(u-1);
    #pragma unroll
    for(int kk=0;kk<36;kk++){
      const int kp = kk+PD;
      if(kp==20) wadv(5*8192);
      if(kp==36) wadv(ODD ? -14*8192 : 4*8192);
      pw[kp%(PD+1)] = ldW(kp<20 ? (kp>>2)*8192 : (kp<36 ? ((kp-20)>>2)*8192 : 0), kp&3);
      if(kk==1)  dma16f<(1-ODD)*K1_U+5*8192>(rwa,lbS,voS,kto[5]*WKT);
      if(kk==5)  dma16f<(1-ODD)*K1_U+6*8192>(rwa,lbS,voS,kto[6]*WKT);
      if(kk==9)  dma16f<(1-ODD)*K1_U+7*8192>(rwa,lbS,voS,kto[7]*WKT);
      if(kk==13) dma16f<(1-ODD)*K1_U+8*8192>(rwa,lbS,voS,kto[8]*WKT);
      if(kk==21) dma16f<ODD*K1_U+0*8192>(rwb,lbS,voS,kto[0]*WKT);
      if(kk==24) dma16f<ODD*K1_U+1*8192>(rwb,lbS,voS,kto[1]*WKT);
      if(kk==27) dma16f<ODD*K1_U+2*8192>(rwb,lbS,voS,kto[2]*WKT);
      if(kk==30) dma16f<ODD*K1_U+3*8192>(rwb,lbS,voS,kto[3]*WKT);
      if(kk==33) dma16f<ODD*K1_U+4*8192>(rwb,lbS,voS,kto[4]*WKT);
      SB();
      if(kk==0){ f32x16 z;
        #pragma unroll
        for(int i=0;i<16;i++) z[i]=0.f;
        N = __builtin_amdgcn_mfma_f32_32x32x16_bf16(pw[0], X[0], z, 0,0,0);
        cs = __builtin_amdgcn_mfma_f32_16x16x32_bf16(sel, pw[0], (f32x4){0.f,0.f,0.f,0.f}, 0,0,0); }
      else { N = __builtin_amdgcn_mfma_f32_32x32x16_bf16(pw[kk%(PD+1)], X[kk], N, 0,0,0);
        if((kk&3)==0) cs = __builtin_amdgcn_mfma_f32_16x16x32_bf16(sel, pw[kk%(PD+1)], cs, 0,0,0); }
      if(kk==2) epi_fix(O);
      if(kk>=4) epi_half(O, (kk-4)>>1, kk&1);
      if(kk==34){ put_cs(ODD); rwa = rwb; rwb = wrsrc(u+3); }
      SB();
      if(kk==19){ TS(1); WAITV(4); BARRIER(); TS(2); }
      if(kk==34 && !ODD){ if(u==4) WAITV(0); }
      if(kk==35){ TS(3); WAITV(5); asm volatile("s_waitcnt lgkmcnt(%0)"::"n"(AB ? 1 : 2):"memory"); BARRIER(); }
    }
    TS(10);
    STAMP(0,2+u);
  };
  #pragma unroll 1
  for(int it=0; it<3; it++){
    interval(Q, P, 2*it+1, IC<1>{});
    if(it<2) interval(P, Q, 2*it+2, IC<0>{});
  }
  const rsrc_t rs = mk_rsrc(w2), rd = mk_rsrc(w2img);
  u32x4 v[3];
  #pragma unroll
  for(int i=0;i<3;i++){ int t = L*8 + wid + i*1920; if(t<5184){ int kb = t/36, cp = t - kb*36;
      v[i] = __builtin_amdgcn_raw_buffer_load_b128(rs, r*1152 + hh*16, kb*32*1152 + cp*32, 0); } }
  epi_load(5);
  epi_fix(Q);
  #pragma unroll
  for(int e=0;e<16;e++){ epi_half(Q,e,0); epi_half(Q,e,1); }
  #pragma unroll
  for(int i=0;i<3;i++){ int t = L*8 + wid + i*1920; if(t<5184){ int kb = t/36, cp = t - kb*36; int nt = cp/9, cin = 2*(cp - nt*9);
      __builtin_amdgcn_raw_buffer_store_b128(v[i], rd, hh*512 + ((r ^ (4*hh))*16) + ((nt*144 + kb)*18 + cin)*512, 0, 16); } }
  STAMP(0,10);
}

__global__ __launch_bounds__(512,2) void k1_kernel(const u16* __restrict__ x, const u16* __restrict__ wre, const u16* __restrict__ wim,
    const u16* __restrict__ w1, const u16* __restrict__ b1, const u16* __restrict__ w2,
    char* himg, char* abimg, char* w2img, unsigned* cnt, unsigned long long* stamps){
  extern __shared__ __attribute__((aligned(16))) char smem[];
  const int bid = blockIdx.x; const int xcd = bid&7, idx = bid>>3;
  const int wid = __builtin_amdgcn_readfirstlane(threadIdx.x>>6), lane = threadIdx.x&63;
  STAMP(0,0);
  if(idx>=30){ if(idx==30 && threadIdx.x<32) cnt[(xcd*32+threadIdx.x)*32] = 0u; return; }
  const int L = xcd*30+idx; const int mh = L/120, rem = L - mh*120; const int nb = rem>>3, mb = mh*8 + (rem&7);
  if(nb<12){ if(wid<4) k1_body<false,0>(smem,x,wre,wim,w1,b1,w2,himg,abimg,w2img,mb,nb,L,wid,lane,stamps);
             else      k1_body<false,1>(smem,x,wre,wim,w1,b1,w2,himg,abimg,w2img,mb,nb,L,wid,lane,stamps); }
  else     { if(wid<4) k1_body<true ,0>(smem,x,wre,wim,w1,b1,w2,himg,abimg,w2img,mb,nb,L,wid,lane,stamps);
             else      k1_body<true ,1>(smem,x,wre,wim,w1,b1,w2,himg,abimg,w2img,mb,nb,L,wid,lane,stamps); }
}

constexpr int K2_AB = 128*128;
constexpr int K2_BB = 64*288;
constexpr int K2_ST = K2_AB + K2_BB;
constexpr int K2_LDS = K2_ST*4;

template<int NP_B>
DI void k2_loader(char* smem, const char* Abase, bool lin, const char* Bbase, int T, int lw, int lane){
  const unsigned lbase = (unsigned)(size_t)(LAS char*)smem;
  const rsrc_t rA = mk_rsrc(Abase), rB = mk_rsrc(Bbase);
  unsigned voA[4];
  #pragma unroll
  for(int p=0;p<4;p++){ int row = (lw*4+p)*8 + (lane>>3); int c = (lane&7) ^ ((row>>1)&7);
    voA[p] = lin ? (unsigned)((lw*4+p)*1024 + lane*16) : (unsigned)(row*(SEQ*2) + c*16); }
  const unsigned astep = lin ? 16384u : 128u;
  const int bp0 = lw<2 ? lw*5 : 10 + (lw-2)*4;
  const unsigned voB = bp0*1024 + lane*16;
  constexpr int P = 4+NP_B;
  auto issue = [&](int st){
    unsigned dst = lbase + (st&3)*K2_ST;
    #pragma unroll
    for(int p=0;p<4;p++) dma16(rA, dst + (lw*4+p)*1024, voA[p], st*astep);
    #pragma unroll
    for(int p=0;p<NP_B;p++) dma16(rB, dst + K2_AB + (bp0+p)*1024, voB, st*K2_BB + p*1024);
  };
  issue(0); issue(1); issue(2);
  WAITV(2*P);
  BARRIER();
  for(int t=0;t<T;t++){
    if(t+2<T) WAITV(P); else WAITV(0);
    BARRIER();
    if(t+3<T) issue(t+3);
  }
}

__global__ __launch_bounds__(512,2) void k2_kernel(const u16* __restrict__ x, const u16* __restrict__ cr, const u16* __restrict__ ci,
    const char* abimg, const char* himg, const char* w2img, const u16* __restrict__ b2,
    float* slab, unsigned* cnt, u16* out, unsigned long long* stamps){
  extern __shared__ __attribute__((aligned(16))) char smem[];
  const int bid = blockIdx.x; const int xcd = bid&7, idx = bid>>3;
  const int sp = xcd>>1;
  const int mt = (xcd&1)*8 + (idx>>2), nt = idx&3;
  const int tile = mt*4+nt;
  const int wid = __builtin_amdgcn_readfirstlane(threadIdx.x>>6), lane = threadIdx.x&63;
  const int T = sp<2 ? 36 : 32;
  f32x4 acc[4][9];
  STAMP(1,0);
  if(wid>=4){
    const char* Abase; const char* Bbase;
    if(sp<2){ Abase = himg + (size_t)(mt*72 + sp*36)*16384; Bbase = w2img + (size_t)(nt*144 + sp*72)*9216; }
    else { Abase = (const char*)((sp==2?cr:ci) + (size_t)mt*128*SEQ); Bbase = abimg + (size_t)(((sp-2)*4+nt)*64)*9216; }
    if(wid<6) k2_loader<5>(smem, Abase, sp<2, Bbase, T, wid-4, lane);
    else      k2_loader<4>(smem, Abase, sp<2, Bbase, T, wid-4, lane);
  } else {
    const int mi = wid>>1, ki = wid&1;
    const int fr = lane&15, g = lane>>4;
    int aoff0, aoff1;
    if(sp<2){ aoff0 = 2*mi*4096 + (4*ki+g)*512 + fr*16; aoff1 = aoff0 + 256; }
    else { aoff0 = (64*mi + fr)*128 + (((4*ki+g) ^ (fr>>1))*16); aoff1 = aoff0 + 2048; }
    const int q = fr>>2, p = lane&3, ph = p>>1;
    const int boff0 = K2_AB + ki*9216 + ph*512 + (8*g + 4*ph + q)*16 + (p&1)*8;
    const int boff1 = K2_AB + ki*9216 + ph*512 + (8*g + 4*(1-ph) + q)*16 + (p&1)*8;
    #pragma unroll
    for(int m=0;m<4;m++)
      #pragma unroll
      for(int n=0;n<9;n++) acc[m][n] = (f32x4){0.f,0.f,0.f,0.f};
    bf16x8 A0[4], A1[4], B[3];
    auto ldA = [&](bf16x8 (&A)[4], int st){ const char* p0 = smem + (st&3)*K2_ST;
      A[0] = *(const bf16x8*)(p0 + aoff0); A[1] = *(const bf16x8*)(p0 + aoff1);
      A[2] = *(const bf16x8*)(p0 + aoff0 + 4096); A[3] = *(const bf16x8*)(p0 + aoff1 + 4096); };
    auto ldB = [&](int st, int n)->bf16x8 { char* p0 = smem + (st&3)*K2_ST + n*1024;
      s16x4 lo = __builtin_amdgcn_ds_read_tr16_b64_v4i16((lds_s16x4)(p0 + boff0));
      s16x4 hi = __builtin_amdgcn_ds_read_tr16_b64_v4i16((lds_s16x4)(p0 + boff1));
      return __builtin_shufflevector(lo,hi,0,1,2,3,4,5,6,7); };
    BARRIER();
    STAMP(1,1);
    ldA(A0,0); B[0]=ldB(0,0); B[1]=ldB(0,1);
    auto step = [&](bf16x8 (&Ac)[4], bf16x8 (&An)[4], int t){
      #pragma unroll
      for(int n=0;n<9;n++){
        if(n+2<=8) B[(n+2)%3] = ldB(t,n+2);
        if(n==6) BARRIER();
        bf16x8 Bc = B[n%3];
        if(n==7){ ldA(An,t+1); B[0] = ldB(t+1,0); }
        if(n==8){ B[1] = ldB(t+1,1); }
        SB();
        #pragma unroll
        for(int m=0;m<4;m++) acc[m][n] = __builtin_amdgcn_mfma_f32_16x16x32_bf16(Bc, Ac[m], acc[m][n], 0,0,0);
        SB();
      }
    };
    for(int t=0;t<T;t+=2){ step(A0,A1,t); step(A1,A0,t+1); }
  }
  STAMP(1,2);
  BARRIER();
  if(wid<4){
    const int ki = wid&1;
    char* dst = smem + (wid^1)*18432 + lane*16;
    #pragma unroll
    for(int j=0;j<2;j++)
      #pragma unroll
      for(int n=0;n<9;n++){ f32x4 v = ki ? acc[j][n] : acc[2+j][n]; *(f32x4*)(dst + (j*9+n)*1024) = v; }
  }
  __syncthreads();
  STAMP(1,3);
  f32x4* own = (f32x4*)(smem + 4*18432);
  const rsrc_t rS = mk_rsrc(slab);
  if(wid<4){
    const int ki = wid&1;
    const char* src = smem + wid*18432 + lane*16;
    unsigned so = (unsigned)(((tile*4 + wid)*4 + sp)*18432);
    #pragma unroll
    for(int j=0;j<2;j++)
      #pragma unroll
      for(int n=0;n<9;n++){ f32x4 v = (ki ? acc[2+j][n] : acc[j][n]) + *(const f32x4*)(src + (j*9+n)*1024);
        if(wid==sp) own[(j*9+n)*64+lane] = v;
        else __builtin_amdgcn_raw_buffer_store_b128(__builtin_bit_cast(u32x4,v), rS, lane*16 + so + (j*9+n)*1024, 0, 16); }
    if(wid!=sp){
      WAITV(0);
      if(lane==0) __hip_atomic_fetch_add(cnt + (tile*4+wid)*32, 1u, __ATOMIC_RELAXED, __HIP_MEMORY_SCOPE_AGENT);
    }
  }
  STAMP(1,4);
  const int fr = lane&15, g = lane>>4;
  u32x2 xv[3], bv[3];
  #pragma unroll
  for(int i=0;i<3;i++){ int b = wid + 8*i; if(b<18){ int j = b/9, n = b - j*9;
      int row = mt*128 + 32*sp + 16*j + fr, col = nt*144 + 16*n + 4*g;
      xv[i] = *(const u32x2*)(x + (size_t)row*DM + col); bv[i] = *(const u32x2*)(b2 + col); } }
  if(wid==7){
    unsigned* c = cnt + (tile*4+sp)*32; int spins=0;
    while(__hip_atomic_load(c, __ATOMIC_RELAXED, __HIP_MEMORY_SCOPE_AGENT) < 3u){ __builtin_amdgcn_s_sleep(1); if(++spins > (1<<22)) break; }
  }
  __syncthreads();
  STAMP(1,5);
  {
    f32x4 pp[3][3];
    #pragma unroll
    for(int i=0;i<3;i++){ int b = wid + 8*i; if(b<18){
      #pragma unroll
      for(int s=0;s<3;s++){ int ss = s + (s>=sp ? 1 : 0);
        pp[i][s] = __builtin_bit_cast(f32x4, __builtin_amdgcn_raw_buffer_load_b128(rS, lane*16, (unsigned)(((tile*4 + sp)*4 + ss)*18432) + b*1024, 16)); } } }
    #pragma unroll
    for(int i=0;i<3;i++){ int b = wid + 8*i; if(b<18){
      f32x4 ow = own[b*64+lane];
      f32x4 mm, sv;
      if(sp<2){ mm = ow + pp[i][0]; sv = pp[i][1] + pp[i][2]; }
      else    { mm = pp[i][0] + pp[i][1]; sv = ow + pp[i][2]; }
      int j = b/9, n = b - j*9;
      int row = mt*128 + 32*sp + 16*j + fr, col = nt*144 + 16*n + 4*g;
      float xf[4] = {__uint_as_float(xv[i].x<<16), __uint_as_float(xv[i].x&0xffff0000u), __uint_as_float(xv[i].y<<16), __uint_as_float(xv[i].y&0xffff0000u)};
      float bf[4] = {__uint_as_float(bv[i].x<<16), __uint_as_float(bv[i].x&0xffff0000u), __uint_as_float(bv[i].y<<16), __uint_as_float(bv[i].y&0xffff0000u)};
      float o[4];
      #pragma unroll
      for(int e=0;e<4;e++){ float m = mm[e]+bf[e];
        float gt = m * __builtin_amdgcn_rcpf(1.f + __builtin_amdgcn_exp2f(-1.44269504f*m));
        o[e] = xf[e] + sv[e]*gt; }
      *(u32x2*)(out + (size_t)row*DM + col) = (u32x2){pk_bf16(o[0],o[1]), pk_bf16(o[2],o[3])};
    } }
  }
  STAMP(1,6);
}

extern "C" void kernel_launch(void* const* d_in, const int* in_sizes, int n_in,
                              void* d_out, int out_size, void* d_ws, size_t ws_size,
                              hipStream_t stream) {
  const u16* x=(const u16*)d_in[0]; const u16* cr=(const u16*)d_in[1]; const u16* ci=(const u16*)d_in[2];
  const u16* wre=(const u16*)d_in[3]; const u16* wim=(const u16*)d_in[4];
  const u16* w1=(const u16*)d_in[6]; const u16* b1=(const u16*)d_in[7]; const u16* w2=(const u16*)d_in[8]; const u16* b2=(const u16*)d_in[9];
  char* ws=(char*)d_ws;
  char* himg = ws; char* abimg = ws + (20<<20); char* w2img = ws + (26<<20);
  float* slab=(float*)(ws+(32<<20)); unsigned* cnt=(unsigned*)(ws+(52<<20));
  unsigned long long* stamps = (unsigned long long*)(ws+(128<<20));
  (void)hipFuncSetAttribute((const void*)k1_kernel, hipFuncAttributeMaxDynamicSharedMemorySize, K1_LDS);
  (void)hipFuncSetAttribute((const void*)k2_kernel, hipFuncAttributeMaxDynamicSharedMemorySize, K2_LDS);
  k1_kernel<<<256,512,K1_LDS,stream>>>(x,wre,wim,w1,b1,w2,himg,abimg,w2img,cnt,stamps);
  k2_kernel<<<256,512,K2_LDS,stream>>>(x,cr,ci,abimg,himg,w2img,b2,slab,cnt,(u16*)d_out,stamps);
}
```

```cpp
#define STAMPS
#define STAMPW 1
#include <hip/hip_runtime.h>
#include <hip/hip_bf16.h>
typedef unsigned short u16;
typedef short bf16x8 __attribute__((ext_vector_type(8)));
typedef short s16x4 __attribute__((ext_vector_type(4)));
typedef float f32x4 __attribute__((ext_vector_type(4)));
typedef float f32x2 __attribute__((ext_vector_type(2)));
typedef float f32x16 __attribute__((ext_vector_type(16)));
typedef unsigned u32x4 __attribute__((ext_vector_type(4)));
typedef unsigned u32x2 __attribute__((ext_vector_type(2)));
typedef __bf16 bf2 __attribute__((ext_vector_type(2)));
#define LAS __attribute__((address_space(3)))
typedef LAS s16x4* lds_s16x4;
typedef LAS void* lds_vp;
typedef __amdgpu_buffer_rsrc_t rsrc_t;
#define DI __device__ __forceinline__
DI float bf2f(u16 v){ return __uint_as_float(((unsigned)v)<<16); }
DI u16 f2bf(float f){ unsigned u=__float_as_uint(f); u += 0x7fffu + ((u>>16)&1u); return (u16)(u>>16); }
DI unsigned pk_bf16(float lo, float hi){ bf2 v; v[0]=(__bf16)lo; v[1]=(__bf16)hi; return __builtin_bit_cast(unsigned,v); }
DI float opq(float x){ asm("" : "+v"(x)); return x; }
DI rsrc_t mk_rsrc(const void* p){ return __builtin_amdgcn_make_buffer_rsrc((void*)p, 0, 0x7fffffff, 0x00020000); }
#define ROT 0
#define PRIO 1
#define PFD 3
constexpr int SEQ=2048, DM=576, FF=4608;

#define STAMP(K,i) do{ if(lane==0 && (wid==STAMPW)) stamps[((K)*256+blockIdx.x)*16+(i)] = __builtin_amdgcn_s_memrealtime(); }while(0)
#define SB() __builtin_amdgcn_sched_barrier(0)
#define BARRIER() do{ SB(); asm volatile("":::"memory"); __builtin_amdgcn_s_barrier(); asm volatile("":::"memory"); SB(); }while(0)
#define WAITV(n) asm volatile("s_waitcnt vmcnt(%0)"::"n"(n):"memory")
#define WAITL0() asm volatile("s_waitcnt lgkmcnt(0)":::"memory")

DI void dma16(rsrc_t r, unsigned lds_addr, unsigned voff, unsigned soff){
  asm volatile("s_mov_b32 m0, %0\n\ts_nop 4\n\tbuffer_load_dwordx4 %1, %2, %3 offen lds"
               :: "s"(lds_addr), "v"(voff), "s"(r), "s"(soff) : "memory");
}

template<int LOFF, int SOFF>
DI void dma16i(rsrc_t r, unsigned lds_base, unsigned voff){
  unsigned tmp;
  asm volatile("s_add_u32 m0, %1, %2\n\ts_mov_b32 %0, %3\n\ts_nop 3\n\tbuffer_load_dwordx4 %4, %5, %0 offen lds"
               : "=&s"(tmp) : "s"(lds_base), "i"(LOFF), "i"(SOFF), "v"(voff), "s"(r) : "memory", "scc");
}
template<int N> struct IC { static constexpr int v = N; };
constexpr int K1_U = 73728;
constexpr int K1_STAT = 2*K1_U;
constexpr int K1_B1   = K1_STAT + 2048;
constexpr int K1_CSP  = K1_B1 + 1536;
constexpr int K1_LDS  = K1_CSP + 2048;

template<int LOFF>
DI void dma16f(rsrc_t r, unsigned lds_base, unsigned voff, unsigned soff){
  asm volatile("s_add_u32 m0, %0, %1\n\ts_nop 3\n\tbuffer_load_dwordx4 %2, %3, %4 offen lds"
               :: "s"(lds_base), "i"(LOFF), "v"(voff), "s"(r), "s"(soff) : "memory", "scc");
}

template<bool AB, int fb>
DI void k1_body(char* smem, const u16* x, const u16* wre, const u16* wim, const u16* w1, const u16* b1, const u16* w2,
                char* himg, char* abimg, char* w2img, int mb, int nb, int L, int wid, int lane, unsigned long long* stamps){
  const unsigned lbase = (unsigned)(size_t)(LAS char*)smem;
  const int w4 = wid&3;
  const int r = lane&31, hh = lane>>5;
  constexpr int WROW = AB ? 1152 : FF*2;
  constexpr int WKT  = AB ? 128 : 64*FF*2;
  const rsrc_t rX = mk_rsrc(x + (size_t)mb*128*DM);
  const unsigned voR0 = (lane>>3)*1152 + (((lane&7) ^ (lane>>4))*16);
  const unsigned voW0 = AB ? voR0 : (unsigned)((lane>>3)*(FF*2) + (((lane&7) ^ (((lane>>4)&1)<<2))*16));
  auto wrsrc = [&](int u)->rsrc_t {
    const int uc = u<=5 ? u : 5; const u16* p;
    if constexpr (AB){ int ec = (nb-12)*384 + uc*64; p = ec>=576 ? wim + (size_t)(ec-576)*DM : wre + (size_t)ec*DM; }
    else p = w1 + nb*384 + uc*64;
    return __builtin_amdgcn_make_buffer_rsrc((void*)p, 0, u<=5 ? 0x7fffffff : 0, 0x00020000);
  };
  if constexpr (!AB && fb==0){ if(wid<3){
    const rsrc_t rb = mk_rsrc(b1 + nb*384 + wid*128);
    asm volatile("s_mov_b32 m0, %0\n\ts_nop 4\n\tbuffer_load_dword %1, %2, 0 offen lds" :: "s"(lbase + K1_B1 + wid*256), "v"(lane*4), "s"(rb) : "memory"); } }
  const unsigned voXa = voR0 + wid*16*1152, voXb = (voR0^64) + (wid*16+8)*1152;
  const unsigned voS = (AB ? (voW0 ^ ((wid&1)<<6)) : voW0) + wid*8*WROW;
  const unsigned lbX = lbase + wid*2048, lbS = lbase + wid*1024;
  unsigned kto[9];
  { const int rho = ROT ? (mb + 3*nb + (nb/3)) % 9 : 0;
    #pragma unroll
    for(int i=0;i<9;i++){ int t = i + rho; if(t>=9) t -= 9; kto[i] = t; } }
  #define DMAX(kt) do{ dma16f<((3*(kt))%18)*8192>(rX, lbX, voXa, kto[kt]*128); dma16f<((3*(kt))%18)*8192+1024>(rX, lbX, voXb, kto[kt]*128); }while(0)
  #define DMAW(rw,slot,kt) dma16f<(slot)*8192>(rw, lbS, voS, kto[kt]*WKT)
  const rsrc_t rw0 = wrsrc(0), rw1 = wrsrc(1), rw2 = wrsrc(2);
  DMAX(0); DMAW(rw0,2,0); DMAX(1); DMAW(rw0,5,1); DMAX(2); DMAW(rw0,8,2);

  unsigned xoff[4];
  unsigned woff[4];
  #pragma unroll
  for(int sp=0;sp<4;sp++){
    int s = sp ^ w4;
    int row = 32*w4 + r;
    xoff[sp] = lbase + row*128 + (((2*s+hh) ^ ((row>>1)&7))<<4);
    if constexpr (AB){ int wr = 32*fb + r; woff[sp] = wr*128 + (((2*s+hh) ^ ((wr>>1)&7))<<4); }
    else { int q=(lane&15)>>2, p=lane&3, blk=(lane>>4)&1; int wr = 16*s + 8*hh + q;
           woff[sp] = wr*128 + ((64*fb + 32*blk + 8*p) ^ (((q>>1)&1)<<6)); }
    woff[sp] += lbase;
  }
  auto wadv = [&](int d){
    #pragma unroll
    for(int sp=0;sp<4;sp++){ woff[sp] += d; asm volatile("" : "+v"(woff[sp])); } };
  auto ldW = [&](int off, int sp)->bf16x8 {
    LAS char* p = (LAS char*)(woff[sp]) + off;
    if constexpr (AB){ return *(const LAS bf16x8*)p; }
    else { s16x4 lo = __builtin_amdgcn_ds_read_tr16_b64_v4i16((lds_s16x4)p);
           s16x4 hi = __builtin_amdgcn_ds_read_tr16_b64_v4i16((lds_s16x4)(p + 512));
           return __builtin_shufflevector(lo,hi,0,1,2,3,4,5,6,7); }
  };
  const short selv = ((lane&15) == ((lane>>4)&1)) ? (short)0x3f80 : (short)0;
  const bf16x8 sel = {selv,selv,selv,selv,selv,selv,selv,selv};

  bf16x8 X[36];
  f32x16 P, Q;
  f32x4 cs = {0.f,0.f,0.f,0.f}, sx = {0.f,0.f,0.f,0.f};
  #pragma unroll
  for(int i=0;i<16;i++){ P[i]=0.f; Q[i]=0.f; }
  constexpr int PD = PFD;
  bf16x8 pw[PD+1];
  STAMP(0,1);
  #define TW(i)
  TW(0);
  if constexpr (fb==1) __builtin_amdgcn_s_setprio(1);
  bf16x8 wf[2][4];
  auto smma = [&](int kt, int h0){
    #pragma unroll
    for(int sp=0;sp<4;sp++){
      P = __builtin_amdgcn_mfma_f32_32x32x16_bf16(wf[kt&1][sp], X[kt*4+sp], P, 0,0,0);
      if(sp==0) cs = __builtin_amdgcn_mfma_f32_16x16x32_bf16(sel, wf[kt&1][0], cs, 0,0,0);
      if((sp>>1)==h0){ Q = __builtin_amdgcn_mfma_f32_32x32x16_bf16(X[kt*4+sp], X[kt*4+sp], Q, 0,0,0);
                       sx = __builtin_amdgcn_mfma_f32_16x16x32_bf16(sel, X[kt*4+sp], sx, 0,0,0); }
    }
    asm volatile("" : "+v"(Q));
    asm volatile("" : "+v"(P), "+v"(sx), "+v"(cs));
  };
  auto sstep = [&](auto KT){
    constexpr int kt = decltype(KT)::v;
    if(kt==3||kt==4) TW(10+3*(kt-3));
    WAITV(6);
    WAITL0();
    if(kt==3||kt==4) TW(11+3*(kt-3));
    BARRIER();
    TW(1+kt);
    #pragma unroll
    for(int sp=0;sp<4;sp++){ X[kt*4+sp] = *(const LAS bf16x8*)((LAS char*)(xoff[sp]) + ((kt&1) ? 24576 : 0)); wf[kt&1][sp] = ldW(16384,sp); }
    SB();
    auto dmas = [&](){
      if constexpr (kt<=5){ DMAX(kt+3); DMAW(rw0,(3*(kt+3)+2)%18,kt+3); }
      else { DMAW(rw1,9+3*(kt-6),3*(kt-6)); DMAW(rw1,10+3*(kt-6),3*(kt-6)+1); DMAW(rw1,11+3*(kt-6),3*(kt-6)+2); }
    };
    if constexpr (kt==0) dmas();
    else if constexpr (fb==0){ dmas(); SB(); smma(kt-1,0); }
    else     { smma(kt-1,1); SB(); dmas(); }
    wadv(kt==5 ? -5*24576 : 24576);
    if(kt&1){
      #pragma unroll
      for(int sp=0;sp<4;sp++){ xoff[sp] += (kt==5 ? -4*24576 : 2*24576); asm volatile("" : "+v"(xoff[sp])); } }
  };
  sstep(IC<0>{}); sstep(IC<1>{}); sstep(IC<2>{}); sstep(IC<3>{}); sstep(IC<4>{}); sstep(IC<5>{}); sstep(IC<6>{}); sstep(IC<7>{}); sstep(IC<8>{});
  smma(8,fb);
  if constexpr (fb==1) __builtin_amdgcn_s_setprio(0);
  auto put_cs = [&](int par){
    if(lane<16){ float* d = (float*)(smem + K1_CSP + par*1024 + wid*128 + lane*4); d[0] = cs[0]; d[16] = cs[1]; }
  };
  put_cs(0);
  if(lane<16){ float* d = (float*)(smem + K1_STAT + fb*512 + (32*w4+lane)*4); d[0] = sx[0]; d[16] = sx[1]; }
  { const int dr = (r&3) + 4*(r>>3); float dg = Q[0];
    #pragma unroll
    for(int i=1;i<16;i++) dg = (dr==i) ? Q[i] : dg;
    if(hh == ((r>>2)&1)) *(float*)(smem + K1_STAT + 1024 + fb*512 + (32*w4+r)*4) = dg; }
  WAITV(0); WAITL0();
  BARRIER();
  STAMP(0,2);
  DMAW(rw2,0,0); DMAW(rw2,1,1); DMAW(rw2,2,2); DMAW(rw2,3,3); DMAW(rw2,4,4);
  #pragma unroll
  for(int i=0;i<PD;i++) pw[i] = ldW(0,i);
  float rr, irr, K0, K1c; bf16x8 Bext;
  { const float* sp_ = (const float*)(smem + K1_STAT + (32*w4+r)*4); const float s1 = sp_[0] + sp_[128], s2 = sp_[256] + sp_[384];
    float mu = s1*(1.f/DM); float var = fmaxf(s2*(1.f/DM) - mu*mu, 0.f);
    float sd = sqrtf(var + 1e-5f);
    irr = sd;
    if constexpr (!AB){ float v2 = var/(var + 1e-5f); irr = sd*sqrtf(v2 + 1e-5f); }
    rr = 1.f/irr;
    K0 = -2.30220820f*rr; K1c = -0.10294324f*rr*rr*rr;
    unsigned i0 = pk_bf16(irr,irr) & 0xffff0000u; unsigned m0 = pk_bf16(-mu,-mu) & 0xffff0000u;
    unsigned wi = pk_bf16(irr, irr - __uint_as_float(i0)), wm = pk_bf16(-mu, -mu - __uint_as_float(m0));
    u32x4 bx = {wi, wm, wm, 0u};
    if(hh) bx = (u32x4){0u,0u,0u,0u};
    Bext = __builtin_bit_cast(bf16x8, bx); }
  const rsrc_t rH = mk_rsrc(AB ? abimg : himg);

  float cp4[4]; unsigned bw = 0; float tz, to[2]; u32x2 pk[2];
  unsigned obase; float a_r = rr; int ce0=0, ri=0;
  auto epi_load = [&](int u){
    if constexpr (AB){ int ec = (nb-12)*384 + u*64; ri = ec>=576; ce0 = ((ec - ri*576)>>3) + 4*fb; a_r = ri ? -rr : rr; obase = hh*512 + ((r ^ (4*hh))*16); }
    else { obase = (unsigned)(((mb*72 + nb*6 + u)*4 + w4)*4096 + fb*2048) + lane*16;
           bw = *(const u16*)(smem + K1_B1 + u*128 + fb*64 + r*2); }
    const float* cp = (const float*)(smem + K1_CSP + (u&1)*1024 + fb*512);
    cp4[0] = cp[r]; cp4[1] = cp[32+r]; cp4[2] = cp[64+r]; cp4[3] = cp[96+r];
  };
  auto epi_fix = [&](f32x16& O){
    float c = opq(opq(cp4[0]+cp4[1]) + opq(cp4[2]+cp4[3]));
    unsigned c0 = pk_bf16(c,c); float c1 = c - __uint_as_float(c0 & 0xffff0000u);
    u32x4 ax = {bw*0x10001u, c0, pk_bf16(c1,c1), 0u};
    O = __builtin_amdgcn_mfma_f32_32x32x16_bf16(__builtin_bit_cast(bf16x8,ax), Bext, O, 0,0,0);
  };
  auto epi_half = [&](const f32x16& O, int e, int half){
    if(half==0){
      if constexpr (!AB){ float v = O[e]; tz = opq(__builtin_amdgcn_exp2f(opq(v*opq(fmaf(opq(v*v), K1c, K0))))); }
    } else {
      if constexpr (AB) to[e&1] = opq(a_r*O[e]);
      else to[e&1] = opq(O[e]*opq(__builtin_amdgcn_rcpf(opq(fmaf(tz, irr, irr)))));
      if(e&1) pk[(e>>2)&1][(e>>1)&1] = pk_bf16(to[0],to[1]);
      if((e&7)==7){
        auto s0 = __builtin_amdgcn_permlane32_swap(pk[0][0], pk[1][0], false, false);
        auto s1 = __builtin_amdgcn_permlane32_swap(pk[0][1], pk[1][1], false, false);
        u32x4 v = {s0[0], s1[0], s0[1], s1[1]};
        unsigned so;
        if constexpr (AB){ int ce = ce0 + (e>>3)*2; int nt = ce/18, cin = ce - nt*18; so = (unsigned)((((ri*4+nt)*64 + mb*4 + w4)*18 + cin)*512); }
        else so = (e>>3)*1024;
        __builtin_amdgcn_raw_buffer_store_b128(v, rH, obase + so, 0, 16);
      }
    }
  };
  #define TS(i)
  rsrc_t rwa = rw2, rwb = wrsrc(3);
  auto interval = [&](f32x16& N, f32x16& O, int u, auto ODDC){
    constexpr int ODD = decltype(ODDC)::v;
    TS(0);
    epi_load(u-1);
    #pragma unroll
    for(int kk=0;kk<36;kk++){
      const int kp = kk+PD;
      if(kp==20) wadv(5*8192);
      if(kp==36) wadv(ODD ? -14*8192 : 4*8192);
      pw[kp%(PD+1)] = ldW(kp<20 ? (kp>>2)*8192 : (kp<36 ? ((kp-20)>>2)*8192 : 0), kp&3);
      if(kk==1)  dma16f<(1-ODD)*K1_U+5*8192>(rwa,lbS,voS,kto[5]*WKT);
      if(kk==5)  dma16f<(1-ODD)*K1_U+6*8192>(rwa,lbS,voS,kto[6]*WKT);
      if(kk==9)  dma16f<(1-ODD)*K1_U+7*8192>(rwa,lbS,voS,kto[7]*WKT);
      if(kk==13) dma16f<(1-ODD)*K1_U+8*8192>(rwa,lbS,voS,kto[8]*WKT);
      if(kk==21) dma16f<ODD*K1_U+0*8192>(rwb,lbS,voS,kto[0]*WKT);
      if(kk==24) dma16f<ODD*K1_U+1*8192>(rwb,lbS,voS,kto[1]*WKT);
      if(kk==27) dma16f<ODD*K1_U+2*8192>(rwb,lbS,voS,kto[2]*WKT);
      if(kk==30) dma16f<ODD*K1_U+3*8192>(rwb,lbS,voS,kto[3]*WKT);
      if(kk==33) dma16f<ODD*K1_U+4*8192>(rwb,lbS,voS,kto[4]*WKT);
      SB();
      if(kk==0){ f32x16 z;
        #pragma unroll
        for(int i=0;i<16;i++) z[i]=0.f;
        N = __builtin_amdgcn_mfma_f32_32x32x16_bf16(pw[0], X[0], z, 0,0,0);
        cs = __builtin_amdgcn_mfma_f32_16x16x32_bf16(sel, pw[0], (f32x4){0.f,0.f,0.f,0.f}, 0,0,0); }
      else { N = __builtin_amdgcn_mfma_f32_32x32x16_bf16(pw[kk%(PD+1)], X[kk], N, 0,0,0);
        if((kk&3)==0) cs = __builtin_amdgcn_mfma_f32_16x16x32_bf16(sel, pw[kk%(PD+1)], cs, 0,0,0); }
      if(kk==2) epi_fix(O);
      if(kk>=4) epi_half(O, (kk-4)>>1, kk&1);
      if(kk==34){ put_cs(ODD); rwa = rwb; rwb = wrsrc(u+3); }
      SB();
      if(kk==19){ TS(1); WAITV(4); BARRIER(); TS(2); }
      if(kk==34 && !ODD){ if(u==4) WAITV(0); }
      if(kk==35){ TS(3); WAITV(5); asm volatile("s_waitcnt lgkmcnt(%0)"::"n"(AB ? 1 : 2):"memory"); BARRIER(); }
    }
    TS(10);
    STAMP(0,2+u);
  };
  #pragma unroll 1
  for(int it=0; it<3; it++){
    interval(Q, P, 2*it+1, IC<1>{});
    if(it<2) interval(P, Q, 2*it+2, IC<0>{});
  }
  const rsrc_t rs = mk_rsrc(w2), rd = mk_rsrc(w2img);
  u32x4 v[3];
  #pragma unroll
  for(int i=0;i<3;i++){ int t = L*8 + wid + i*1920; if(t<5184){ int kb = t/36, cp = t - kb*36;
      v[i] = __builtin_amdgcn_raw_buffer_load_b128(rs, r*1152 + hh*16, kb*32*1152 + cp*32, 0); } }
  epi_load(5);
  epi_fix(Q);
  #pragma unroll
  for(int e=0;e<16;e++){ epi_half(Q,e,0); epi_half(Q,e,1); }
  #pragma unroll
  for(int i=0;i<3;i++){ int t = L*8 + wid + i*1920; if(t<5184){ int kb = t/36, cp = t - kb*36; int nt = cp/9, cin = 2*(cp - nt*9);
      __builtin_amdgcn_raw_buffer_store_b128(v[i], rd, hh*512 + ((r ^ (4*hh))*16) + ((nt*144 + kb)*18 + cin)*512, 0, 16); } }
  STAMP(0,10);
}

__global__ __launch_bounds__(512,2) void k1_kernel(const u16* __restrict__ x, const u16* __restrict__ wre, const u16* __restrict__ wim,
    const u16* __restrict__ w1, const u16* __restrict__ b1, const u16* __restrict__ w2,
    char* himg, char* abimg, char* w2img, unsigned* cnt, unsigned long long* stamps){
  extern __shared__ __attribute__((aligned(16))) char smem[];
  const int bid = blockIdx.x; const int xcd = bid&7, idx = bid>>3;
  const int wid = __builtin_amdgcn_readfirstlane(threadIdx.x>>6), lane = threadIdx.x&63;
  STAMP(0,0);
  if(idx>=30){ if(idx==30 && threadIdx.x<32) cnt[(xcd*32+threadIdx.x)*32] = 0u; return; }
  const int L = xcd*30+idx; const int mh = L/120, rem = L - mh*120; const int nb = rem>>3, mb = mh*8 + (rem&7);
  if(nb<12){ if(wid<4) k1_body<false,0>(smem,x,wre,wim,w1,b1,w2,himg,abimg,w2img,mb,nb,L,wid,lane,stamps);
             else      k1_body<false,1>(smem,x,wre,wim,w1,b1,w2,himg,abimg,w2img,mb,nb,L,wid,lane,stamps); }
  else     { if(wid<4) k1_body<true ,0>(smem,x,wre,wim,w1,b1,w2,himg,abimg,w2img,mb,nb,L,wid,lane,stamps);
             else      k1_body<true ,1>(smem,x,wre,wim,w1,b1,w2,himg,abimg,w2img,mb,nb,L,wid,lane,stamps); }
}

constexpr int K2_AB = 128*128;
constexpr int K2_BB = 64*288;
constexpr int K2_ST = K2_AB + K2_BB;
constexpr int K2_LDS = K2_ST*4;

template<int NP_B>
DI void k2_loader(char* smem, const char* Abase, bool lin, const char* Bbase, int T, int lw, int lane){
  const unsigned lbase = (unsigned)(size_t)(LAS char*)smem;
  const rsrc_t rA = mk_rsrc(Abase), rB = mk_rsrc(Bbase);
  unsigned voA[4];
  #pragma unroll
  for(int p=0;p<4;p++){ int row = (lw*4+p)*8 + (lane>>3); int c = (lane&7) ^ ((row>>1)&7);
    voA[p] = lin ? (unsigned)((lw*4+p)*1024 + lane*16) : (unsigned)(row*(SEQ*2) + c*16); }
  const unsigned astep = lin ? 16384u : 128u;
  const int bp0 = lw<2 ? lw*5 : 10 + (lw-2)*4;
  const unsigned voB = bp0*1024 + lane*16;
  constexpr int P = 4+NP_B;
  auto issue = [&](int st){
    unsigned dst = lbase + (st&3)*K2_ST;
    #pragma unroll
    for(int p=0;p<4;p++) dma16(rA, dst + (lw*4+p)*1024, voA[p], st*astep);
    #pragma unroll
    for(int p=0;p<NP_B;p++) dma16(rB, dst + K2_AB + (bp0+p)*1024, voB, st*K2_BB + p*1024);
  };
  issue(0); issue(1); issue(2);
  WAITV(2*P);
  BARRIER();
  for(int t=0;t<T;t++){
    if(t+2<T) WAITV(P); else WAITV(0);
    BARRIER();
    if(t+3<T) issue(t+3);
  }
}

__global__ __launch_bounds__(512,2) void k2_kernel(const u16* __restrict__ x, const u16* __restrict__ cr, const u16* __restrict__ ci,
    const char* abimg, const char* himg, const char* w2img, const u16* __restrict__ b2,
    float* slab, unsigned* cnt, u16* out, unsigned long long* stamps){
  extern __shared__ __attribute__((aligned(16))) char smem[];
  const int bid = blockIdx.x; const int xcd = bid&7, idx = bid>>3;
  const int sp = xcd>>1;
  const int mt = (xcd&1)*8 + (idx>>2), nt = idx&3;
  const int tile = mt*4+nt;
  const int wid = __builtin_amdgcn_readfirstlane(threadIdx.x>>6), lane = threadIdx.x&63;
  const int T = sp<2 ? 36 : 32;
  f32x4 acc[4][9];
  STAMP(1,0);
  if(wid>=4){
    const char* Abase; const char* Bbase;
    if(sp<2){ Abase = himg + (size_t)(mt*72 + sp*36)*16384; Bbase = w2img + (size_t)(nt*144 + sp*72)*9216; }
    else { Abase = (const char*)((sp==2?cr:ci) + (size_t)mt*128*SEQ); Bbase = abimg + (size_t)(((sp-2)*4+nt)*64)*9216; }
    if(wid<6) k2_loader<5>(smem, Abase, sp<2, Bbase, T, wid-4, lane);
    else      k2_loader<4>(smem, Abase, sp<2, Bbase, T, wid-4, lane);
  } else {
    const int mi = wid>>1, ki = wid&1;
    const int fr = lane&15, g = lane>>4;
    int aoff0, aoff1;
    if(sp<2){ aoff0 = 2*mi*4096 + (4*ki+g)*512 + fr*16; aoff1 = aoff0 + 256; }
    else { aoff0 = (64*mi + fr)*128 + (((4*ki+g) ^ (fr>>1))*16); aoff1 = aoff0 + 2048; }
    const int q = fr>>2, p = lane&3, ph = p>>1;
    const int boff0 = K2_AB + ki*9216 + ph*512 + (8*g + 4*ph + q)*16 + (p&1)*8;
    const int boff1 = K2_AB + ki*9216 + ph*512 + (8*g + 4*(1-ph) + q)*16 + (p&1)*8;
    #pragma unroll
    for(int m=0;m<4;m++)
      #pragma unroll
      for(int n=0;n<9;n++) acc[m][n] = (f32x4){0.f,0.f,0.f,0.f};
    bf16x8 A0[4], A1[4], B[3];
    auto ldA = [&](bf16x8 (&A)[4], int st){ const char* p0 = smem + (st&3)*K2_ST;
      A[0] = *(const bf16x8*)(p0 + aoff0); A[1] = *(const bf16x8*)(p0 + aoff1);
      A[2] = *(const bf16x8*)(p0 + aoff0 + 4096); A[3] = *(const bf16x8*)(p0 + aoff1 + 4096); };
    auto ldB = [&](int st, int n)->bf16x8 { char* p0 = smem + (st&3)*K2_ST + n*1024;
      s16x4 lo = __builtin_amdgcn_ds_read_tr16_b64_v4i16((lds_s16x4)(p0 + boff0));
      s16x4 hi = __builtin_amdgcn_ds_read_tr16_b64_v4i16((lds_s16x4)(p0 + boff1));
      return __builtin_shufflevector(lo,hi,0,1,2,3,4,5,6,7); };
    BARRIER();
    STAMP(1,1);
    ldA(A0,0); B[0]=ldB(0,0); B[1]=ldB(0,1);
    auto step = [&](bf16x8 (&Ac)[4], bf16x8 (&An)[4], int t){
      #pragma unroll
      for(int n=0;n<9;n++){
        if(n+2<=8) B[(n+2)%3] = ldB(t,n+2);
        if(n==6) BARRIER();
        bf16x8 Bc = B[n%3];
        if(n==7){ ldA(An,t+1); B[0] = ldB(t+1,0); }
        if(n==8){ B[1] = ldB(t+1,1); }
        SB();
        #pragma unroll
        for(int m=0;m<4;m++) acc[m][n] = __builtin_amdgcn_mfma_f32_16x16x32_bf16(Bc, Ac[m], acc[m][n], 0,0,0);
        SB();
      }
    };
    for(int t=0;t<T;t+=2){ step(A0,A1,t); step(A1,A0,t+1); }
  }
  STAMP(1,2);
  BARRIER();
  if(wid<4){
    const int ki = wid&1;
    char* dst = smem + (wid^1)*18432 + lane*16;
    #pragma unroll
    for(int j=0;j<2;j++)
      #pragma unroll
      for(int n=0;n<9;n++){ f32x4 v = ki ? acc[j][n] : acc[2+j][n]; *(f32x4*)(dst + (j*9+n)*1024) = v; }
  }
  __syncthreads();
  STAMP(1,3);
  f32x4* own = (f32x4*)(smem + 4*18432);
  const rsrc_t rS = mk_rsrc(slab);
  if(wid<4){
    const int ki = wid&1;
    const char* src = smem + wid*18432 + lane*16;
    unsigned so = (unsigned)(((tile*4 + wid)*4 + sp)*18432);
    #pragma unroll
    for(int j=0;j<2;j++)
      #pragma unroll
      for(int n=0;n<9;n++){ f32x4 v = (ki ? acc[2+j][n] : acc[j][n]) + *(const f32x4*)(src + (j*9+n)*1024);
        if(wid==sp) own[(j*9+n)*64+lane] = v;
        else __builtin_amdgcn_raw_buffer_store_b128(__builtin_bit_cast(u32x4,v), rS, lane*16 + so + (j*9+n)*1024, 0, 16); }
    if(wid!=sp){
      WAITV(0);
      if(lane==0) __hip_atomic_fetch_add(cnt + (tile*4+wid)*32, 1u, __ATOMIC_RELAXED, __HIP_MEMORY_SCOPE_AGENT);
    }
  }
  STAMP(1,4);
  const int fr = lane&15, g = lane>>4;
  u32x2 xv[3], bv[3];
  #pragma unroll
  for(int i=0;i<3;i++){ int b = wid + 8*i; if(b<18){ int j = b/9, n = b - j*9;
      int row = mt*128 + 32*sp + 16*j + fr, col = nt*144 + 16*n + 4*g;
      xv[i] = *(const u32x2*)(x + (size_t)row*DM + col); bv[i] = *(const u32x2*)(b2 + col); } }
  if(wid==7){
    unsigned* c = cnt + (tile*4+sp)*32; int spins=0;
    while(__hip_atomic_load(c, __ATOMIC_RELAXED, __HIP_MEMORY_SCOPE_AGENT) < 3u){ __builtin_amdgcn_s_sleep(1); if(++spins > (1<<22)) break; }
  }
  __syncthreads();
  STAMP(1,5);
  {
    f32x4 pp[3][3];
    #pragma unroll
    for(int i=0;i<3;i++){ int b = wid + 8*i; if(b<18){
      #pragma unroll
      for(int s=0;s<3;s++){ int ss = s + (s>=sp ? 1 : 0);
        pp[i][s] = __builtin_bit_cast(f32x4, __builtin_amdgcn_raw_buffer_load_b128(rS, lane*16, (unsigned)(((tile*4 + sp)*4 + ss)*18432) + b*1024, 16)); } } }
    #pragma unroll
    for(int i=0;i<3;i++){ int b = wid + 8*i; if(b<18){
      f32x4 ow = own[b*64+lane];
      f32x4 mm, sv;
      if(sp<2){ mm = ow + pp[i][0]; sv = pp[i][1] + pp[i][2]; }
      else    { mm = pp[i][0] + pp[i][1]; sv = ow + pp[i][2]; }
      int j = b/9, n = b - j*9;
      int row = mt*128 + 32*sp + 16*j + fr, col = nt*144 + 16*n + 4*g;
      float xf[4] = {__uint_as_float(xv[i].x<<16), __uint_as_float(xv[i].x&0xffff0000u), __uint_as_float(xv[i].y<<16), __uint_as_float(xv[i].y&0xffff0000u)};
      float bf[4] = {__uint_as_float(bv[i].x<<16), __uint_as_float(bv[i].x&0xffff0000u), __uint_as_float(bv[i].y<<16), __uint_as_float(bv[i].y&0xffff0000u)};
      float o[4];
      #pragma unroll
      for(int e=0;e<4;e++){ float m = mm[e]+bf[e];
        float gt = m * __builtin_amdgcn_rcpf(1.f + __builtin_amdgcn_exp2f(-1.44269504f*m));
        o[e] = xf[e] + sv[e]*gt; }
      *(u32x2*)(out + (size_t)row*DM + col) = (u32x2){pk_bf16(o[0],o[1]), pk_bf16(o[2],o[3])};
    } }
  }
  STAMP(1,6);
}

__global__ void rep_kernel(const unsigned long long* st){
  __shared__ double sh[2];
  if(threadIdx.x<2){
    int k = threadIdx.x? 0:0, s = threadIdx.x? 0:0, m = threadIdx.x? 0:0;
    unsigned long long base = st[(256)*16];
    double mn=1e30,mx=-1e30,av=0; int n=0;
    for(int b=0;b<256;b++){ if(k==0 && s>0 && (b>>3)>=30) continue; double v=(double)(long long)(st[(k*256+b)*16+s]-base); mn=fmin(mn,v); mx=fmax(mx,v); av+=v; n++; }
    sh[threadIdx.x] = m==0?mn:(m==1?mx:av/n);
  }
  __syncthreads();
  double d = sh[1]-sh[0]; if(d<0) d=0; if(d>20000) d=20000;
  unsigned long long t0=__builtin_amdgcn_s_memrealtime();
  while((double)(__builtin_amdgcn_s_memrealtime()-t0) < d*100.0) __builtin_amdgcn_s_sleep(10);
}

extern "C" void kernel_launch(void* const* d_in, const int* in_sizes, int n_in,
                              void* d_out, int out_size, void* d_ws, size_t ws_size,
                              hipStream_t stream) {
  const u16* x=(const u16*)d_in[0]; const u16* cr=(const u16*)d_in[1]; const u16* ci=(const u16*)d_in[2];
  const u16* wre=(const u16*)d_in[3]; const u16* wim=(const u16*)d_in[4];
  const u16* w1=(const u16*)d_in[6]; const u16* b1=(const u16*)d_in[7]; const u16* w2=(const u16*)d_in[8]; const u16* b2=(const u16*)d_in[9];
  char* ws=(char*)d_ws;
  char* himg = ws; char* abimg = ws + (20<<20); char* w2img = ws + (26<<20);
  float* slab=(float*)(ws+(32<<20)); unsigned* cnt=(unsigned*)(ws+(52<<20));
  unsigned long long* stamps = (unsigned long long*)(ws+(128<<20));
  (void)hipFuncSetAttribute((const void*)k1_kernel, hipFuncAttributeMaxDynamicSharedMemorySize, K1_LDS);
  (void)hipFuncSetAttribute((const void*)k2_kernel, hipFuncAttributeMaxDynamicSharedMemorySize, K2_LDS);
  k1_kernel<<<256,512,K1_LDS,stream>>>(x,wre,wim,w1,b1,w2,himg,abimg,w2img,cnt,stamps);
  k2_kernel<<<256,512,K2_LDS,stream>>>(x,cr,ci,abimg,himg,w2img,b2,slab,cnt,(u16*)d_out,stamps);
  rep_kernel<<<1,64,0,stream>>>(stamps);
}
```

```cpp
#include <hip/hip_runtime.h>
#include <hip/hip_bf16.h>
typedef unsigned short u16;
typedef short bf16x8 __attribute__((ext_vector_type(8)));
typedef short s16x4 __attribute__((ext_vector_type(4)));
typedef float f32x4 __attribute__((ext_vector_type(4)));
typedef float f32x2 __attribute__((ext_vector_type(2)));
typedef float f32x16 __attribute__((ext_vector_type(16)));
typedef unsigned u32x4 __attribute__((ext_vector_type(4)));
typedef unsigned u32x2 __attribute__((ext_vector_type(2)));
typedef __bf16 bf2 __attribute__((ext_vector_type(2)));
#define LAS __attribute__((address_space(3)))
typedef LAS s16x4* lds_s16x4;
typedef LAS void* lds_vp;
typedef __amdgpu_buffer_rsrc_t rsrc_t;
#define DI __device__ __forceinline__
DI float bf2f(u16 v){ return __uint_as_float(((unsigned)v)<<16); }
DI u16 f2bf(float f){ unsigned u=__float_as_uint(f); u += 0x7fffu + ((u>>16)&1u); return (u16)(u>>16); }
DI unsigned pk_bf16(float lo, float hi){ bf2 v; v[0]=(__bf16)lo; v[1]=(__bf16)hi; return __builtin_bit_cast(unsigned,v); }
DI float opq(float x){ asm("" : "+v"(x)); return x; }
DI rsrc_t mk_rsrc(const void* p){ return __builtin_amdgcn_make_buffer_rsrc((void*)p, 0, 0x7fffffff, 0x00020000); }
#define ROT 0
#define PRIO 1
#define K2BD 3
#define CPF 1
#define PSLEEP 2
#define PFD 3
constexpr int SEQ=2048, DM=576, FF=4608;

#define STAMP(K,i)
#define SB() __builtin_amdgcn_sched_barrier(0)
#define BARRIER() do{ SB(); asm volatile("":::"memory"); __builtin_amdgcn_s_barrier(); asm volatile("":::"memory"); SB(); }while(0)
#define WAITV(n) asm volatile("s_waitcnt vmcnt(%0)"::"n"(n):"memory")
#define WAITL0() asm volatile("s_waitcnt lgkmcnt(0)":::"memory")

DI void dma16(rsrc_t r, unsigned lds_addr, unsigned voff, unsigned soff){
  asm volatile("s_mov_b32 m0, %0\n\ts_nop 4\n\tbuffer_load_dwordx4 %1, %2, %3 offen lds"
               :: "s"(lds_addr), "v"(voff), "s"(r), "s"(soff) : "memory");
}

template<int LOFF, int SOFF>
DI void dma16i(rsrc_t r, unsigned lds_base, unsigned voff){
  unsigned tmp;
  asm volatile("s_add_u32 m0, %1, %2\n\ts_mov_b32 %0, %3\n\ts_nop 3\n\tbuffer_load_dwordx4 %4, %5, %0 offen lds"
               : "=&s"(tmp) : "s"(lds_base), "i"(LOFF), "i"(SOFF), "v"(voff), "s"(r) : "memory", "scc");
}
template<int N> struct IC { static constexpr int v = N; };
template<int B, int E, class Fn> DI void static_for(Fn&& f){ if constexpr (B<E){ f(IC<B>{}); static_for<B+1,E>(f); } }
constexpr int K1_U = 73728;
constexpr int K1_STAT = 2*K1_U;
constexpr int K1_B1   = K1_STAT + 2048;
constexpr int K1_CSP  = K1_B1 + 1536;
constexpr int K1_DUM  = K1_CSP + 2048;
constexpr int K1_LDS  = K1_DUM + 4096;

DI void sentinel_fill(const void* slab, int p, int lane){
  const int tq = p/27, rem = p - tq*27, k3 = rem/9, n = rem - k3*9, q = tq&3, src = k3 + (k3>=q ? 1 : 0);
  const u32x4 ff = {0xffffffffu,0xffffffffu,0xffffffffu,0xffffffffu};
  __builtin_amdgcn_raw_buffer_store_b128(ff, mk_rsrc(slab), (unsigned)((tq*4 + src)*9216 + n*1024) + lane*16, 0, 16);
}
DI void touch_lines(const u16* p, unsigned lds_dummy, int lane){
  const rsrc_t rb = mk_rsrc(p);
  asm volatile("s_mov_b32 m0, %0\n\ts_nop 4\n\tbuffer_load_dword %1, %2, 0 offen lds" :: "s"(lds_dummy), "v"(lane*128), "s"(rb) : "memory");
}
template<bool AB, int fb>
DI void k1_body(char* smem, const u16* x, const u16* wre, const u16* wim, const u16* w1, const u16* b1, const u16* w2,
                char* himg, char* abimg, char* w2img, int mb, int nb, int L, int wid, int lane, unsigned long long* stamps, const u16* cpf, const void* slab){
  const unsigned lbase = (unsigned)(size_t)(LAS char*)smem;
  const int w4 = wid&3;
  const int r = lane&31, hh = lane>>5;
  constexpr int WROW = AB ? 1152 : FF*2;
  constexpr int WKT  = AB ? 128 : 64*FF*2;
  const rsrc_t rX = mk_rsrc(x + (size_t)mb*128*DM);
  const unsigned voR0 = (lane>>3)*1152 + (((lane&7) ^ (lane>>4))*16);
  const unsigned voW0 = AB ? voR0 : (unsigned)((lane>>3)*(FF*2) + (((lane&7) ^ (((lane>>4)&1)<<2))*16));
  auto wrsrc = [&](int u)->rsrc_t {
    const int uc = u<=5 ? u : 5; const u16* p;
    if constexpr (AB){ int ec = (nb-12)*384 + uc*64; p = ec>=576 ? wim + (size_t)(ec-576)*DM : wre + (size_t)ec*DM; }
    else p = w1 + nb*384 + uc*64;
    return __builtin_amdgcn_make_buffer_rsrc((void*)p, 0, u<=5 ? 0x7fffffff : 0, 0x00020000);
  };
  if constexpr (!AB && fb==0){ if(wid<3){
    const rsrc_t rb = mk_rsrc(b1 + nb*384 + wid*128);
    asm volatile("s_mov_b32 m0, %0\n\ts_nop 4\n\tbuffer_load_dword %1, %2, 0 offen lds" :: "s"(lbase + K1_B1 + wid*256), "v"(lane*4), "s"(rb) : "memory"); } }
  const unsigned voXa = voR0 + wid*16*1152, voXb = (voR0^64) + (wid*16+8)*1152;
  const unsigned voS = (AB ? (voW0 ^ ((wid&1)<<6)) : voW0) + wid*8*WROW;
  const unsigned lbX = lbase + wid*2048, lbS = lbase + wid*1024;
  #define DMAX(kt) do{ dma16i<((3*(kt))%18)*8192, (kt)*128>(rX, lbX, voXa); dma16i<((3*(kt))%18)*8192+1024, (kt)*128>(rX, lbX, voXb); }while(0)
  #define DMAW(rw,slot,kt) dma16i<(slot)*8192, (kt)*WKT>(rw, lbS, voS)
  const rsrc_t rw0 = wrsrc(0), rw1 = wrsrc(1), rw2 = wrsrc(2);
  DMAX(0); DMAW(rw0,2,0); DMAX(1); DMAW(rw0,5,1); DMAX(2); DMAW(rw0,8,2);

  unsigned xoff[4];
  unsigned woff[4];
  #pragma unroll
  for(int sp=0;sp<4;sp++){
    int s = sp ^ w4;
    int row = 32*w4 + r;
    xoff[sp] = lbase + row*128 + (((2*s+hh) ^ ((row>>1)&7))<<4);
    if constexpr (AB){ int wr = 32*fb + r; woff[sp] = wr*128 + (((2*s+hh) ^ ((wr>>1)&7))<<4); }
    else { int q=(lane&15)>>2, p=lane&3, blk=(lane>>4)&1; int wr = 16*s + 8*hh + q;
           woff[sp] = wr*128 + ((64*fb + 32*blk + 8*p) ^ (((q>>1)&1)<<6)); }
    woff[sp] += lbase;
  }
  auto wadv = [&](int d){
    #pragma unroll
    for(int sp=0;sp<4;sp++){ woff[sp] += d; asm volatile("" : "+v"(woff[sp])); } };
  auto ldW = [&](int off, int sp)->bf16x8 {
    LAS char* p = (LAS char*)(woff[sp]) + off;
    if constexpr (AB){ return *(const LAS bf16x8*)p; }
    else { s16x4 lo = __builtin_amdgcn_ds_read_tr16_b64_v4i16((lds_s16x4)p);
           s16x4 hi = __builtin_amdgcn_ds_read_tr16_b64_v4i16((lds_s16x4)(p + 512));
           return __builtin_shufflevector(lo,hi,0,1,2,3,4,5,6,7); }
  };
  const short selv = ((lane&15) == ((lane>>4)&1)) ? (short)0x3f80 : (short)0;
  const bf16x8 sel = {selv,selv,selv,selv,selv,selv,selv,selv};

  bf16x8 X[36];
  f32x16 P, Q;
  f32x4 cs = {0.f,0.f,0.f,0.f}, sx = {0.f,0.f,0.f,0.f};
  #pragma unroll
  for(int i=0;i<16;i++){ P[i]=0.f; Q[i]=0.f; }
  constexpr int PD = PFD;
  bf16x8 pw[PD+1];
  STAMP(0,1);
  #define TW(i)
  TW(0);
  if constexpr (fb==1) __builtin_amdgcn_s_setprio(1);
  bf16x8 wf[2][4];
  auto smma = [&](int kt, int h0){
    #pragma unroll
    for(int sp=0;sp<4;sp++){
      P = __builtin_amdgcn_mfma_f32_32x32x16_bf16(wf[kt&1][sp], X[kt*4+sp], P, 0,0,0);
      if(sp==0) cs = __builtin_amdgcn_mfma_f32_16x16x32_bf16(sel, wf[kt&1][0], cs, 0,0,0);
      if((sp>>1)==h0){ Q = __builtin_amdgcn_mfma_f32_32x32x16_bf16(X[kt*4+sp], X[kt*4+sp], Q, 0,0,0);
                       sx = __builtin_amdgcn_mfma_f32_16x16x32_bf16(sel, X[kt*4+sp], sx, 0,0,0); }
    }
    asm volatile("" : "+v"(Q));
    asm volatile("" : "+v"(P), "+v"(sx), "+v"(cs));
  };
  auto sstep = [&](auto KT){
    constexpr int kt = decltype(KT)::v;
    if(kt==3||kt==4) TW(10+3*(kt-3));
    WAITV(6);
    WAITL0();
    if(kt==3||kt==4) TW(11+3*(kt-3));
    BARRIER();
    TW(1+kt);
    #pragma unroll
    for(int sp=0;sp<4;sp++){ X[kt*4+sp] = *(const LAS bf16x8*)((LAS char*)(xoff[sp]) + ((kt&1) ? 24576 : 0)); wf[kt&1][sp] = ldW(16384,sp); }
    SB();
    auto dmas = [&](){
      if constexpr (kt<=5){ DMAX(kt+3); DMAW(rw0,(3*(kt+3)+2)%18,kt+3); }
      else { DMAW(rw1,9+3*(kt-6),3*(kt-6)); DMAW(rw1,10+3*(kt-6),3*(kt-6)+1); DMAW(rw1,11+3*(kt-6),3*(kt-6)+2); }
    };
    if constexpr (kt==0) dmas();
    else if constexpr (fb==0){ dmas(); SB(); smma(kt-1,0); }
    else     { smma(kt-1,1); SB(); dmas(); }
    wadv(kt==5 ? -5*24576 : 24576);
    if(kt&1){
      #pragma unroll
      for(int sp=0;sp<4;sp++){ xoff[sp] += (kt==5 ? -4*24576 : 2*24576); asm volatile("" : "+v"(xoff[sp])); } }
  };
  sstep(IC<0>{}); sstep(IC<1>{}); sstep(IC<2>{}); sstep(IC<3>{}); sstep(IC<4>{}); sstep(IC<5>{}); sstep(IC<6>{}); sstep(IC<7>{}); sstep(IC<8>{});
  smma(8,fb);
  if constexpr (fb==1) __builtin_amdgcn_s_setprio(0);
  auto put_cs = [&](int par){
    if(lane<16){ float* d = (float*)(smem + K1_CSP + par*1024 + wid*128 + lane*4); d[0] = cs[0]; d[16] = cs[1]; }
  };
  put_cs(0);
  if(lane<16){ float* d = (float*)(smem + K1_STAT + fb*512 + (32*w4+lane)*4); d[0] = sx[0]; d[16] = sx[1]; }
  { const int dr = (r&3) + 4*(r>>3); float dg = Q[0];
    #pragma unroll
    for(int i=1;i<16;i++) dg = (dr==i) ? Q[i] : dg;
    if(hh == ((r>>2)&1)) *(float*)(smem + K1_STAT + 1024 + fb*512 + (32*w4+r)*4) = dg; }
  WAITV(0); WAITL0();
  BARRIER();
  STAMP(0,2);
  DMAW(rw2,0,0); DMAW(rw2,1,1); DMAW(rw2,2,2); DMAW(rw2,3,3); DMAW(rw2,4,4);
  #pragma unroll
  for(int i=0;i<PD;i++) pw[i] = ldW(0,i);
  float rr, irr, K0, K1c; bf16x8 Bext;
  { const float* sp_ = (const float*)(smem + K1_STAT + (32*w4+r)*4); const float s1 = sp_[0] + sp_[128], s2 = sp_[256] + sp_[384];
    float mu = s1*(1.f/DM); float var = fmaxf(s2*(1.f/DM) - mu*mu, 0.f);
    float sd = sqrtf(var + 1e-5f);
    irr = sd;
    if constexpr (!AB){ float v2 = var/(var + 1e-5f); irr = sd*sqrtf(v2 + 1e-5f); }
    rr = 1.f/irr;
    K0 = -2.30220820f*rr; K1c = -0.10294324f*rr*rr*rr;
    unsigned i0 = pk_bf16(irr,irr) & 0xffff0000u; unsigned m0 = pk_bf16(-mu,-mu) & 0xffff0000u;
    unsigned wi = pk_bf16(irr, irr - __uint_as_float(i0)), wm = pk_bf16(-mu, -mu - __uint_as_float(m0));
    u32x4 bx = {wi, wm, wm, 0u};
    if(hh) bx = (u32x4){0u,0u,0u,0u};
    Bext = __builtin_bit_cast(bf16x8, bx); }
  const rsrc_t rH = mk_rsrc(AB ? abimg : himg);

  float cp4[4]; unsigned bw = 0; float to[2]; u32x2 pk[2];
  unsigned obase; float a_r = rr; int ce0=0, ri=0;
  auto epi_load = [&](int u){
    if constexpr (AB){ int ec = (nb-12)*384 + u*64; ri = ec>=576; ce0 = ((ec - ri*576)>>3) + 4*fb; a_r = ri ? -rr : rr; obase = hh*512 + ((r ^ (4*hh))*16); }
    else { obase = (unsigned)(((mb*72 + nb*6 + u)*4 + w4)*4096 + fb*2048) + lane*16;
           bw = *(const u16*)(smem + K1_B1 + u*128 + fb*64 + r*2); }
    const float* cp = (const float*)(smem + K1_CSP + (u&1)*1024 + fb*512);
    cp4[0] = cp[r]; cp4[1] = cp[32+r]; cp4[2] = cp[64+r]; cp4[3] = cp[96+r];
  };
  auto epi_fix = [&](f32x16& O){
    float c = opq(opq(cp4[0]+cp4[1]) + opq(cp4[2]+cp4[3]));
    unsigned c0 = pk_bf16(c,c); float c1 = c - __uint_as_float(c0 & 0xffff0000u);
    u32x4 ax = {bw*0x10001u, c0, pk_bf16(c1,c1), 0u};
    O = __builtin_amdgcn_mfma_f32_32x32x16_bf16(__builtin_bit_cast(bf16x8,ax), Bext, O, 0,0,0);
  };
  float tq[3]; u32x4 vst; unsigned ost;
  auto epi_q = [&](const f32x16& O, int e, int stg){
    float& t = tq[e%3];
    if constexpr (AB){
      if(stg==3) to[e&1] = opq(a_r*O[e]);
    } else {
      const float v = O[e];
      if(stg==0) t = opq(fmaf(opq(v*v), K1c, K0));
      if(stg==1) t = opq(__builtin_amdgcn_exp2f(opq(v*t)));
      if(stg==2) t = opq(__builtin_amdgcn_rcpf(opq(fmaf(t, irr, irr))));
      if(stg==3) to[e&1] = opq(v*t);
    }
    if(stg==3){
      if(e&1) pk[(e>>2)&1][(e>>1)&1] = pk_bf16(to[0],to[1]);
      if((e&7)==7){
        auto s0 = __builtin_amdgcn_permlane32_swap(pk[0][0], pk[1][0], false, false);
        auto s1 = __builtin_amdgcn_permlane32_swap(pk[0][1], pk[1][1], false, false);
        u32x4 v = {s0[0], s1[0], s0[1], s1[1]};
        unsigned so;
        if constexpr (AB){ int ce = ce0 + (e>>3)*2; int nt = ce/18, cin = ce - nt*18; so = (unsigned)((((ri*4+nt)*64 + mb*4 + w4)*18 + cin)*512); }
        else so = (e>>3)*1024;
        vst = v; ost = obase + so;
      }
    }
  };
  auto epi_store = [&](){ __builtin_amdgcn_raw_buffer_store_b128(vst, rH, ost, 0, 16); };
  auto epi_step = [&](const f32x16& O, int kk){
    if(kk<4) return;
    const int e1 = (kk-2)>>1, e0 = e1-1;
    if(kk<8) epi_q(O,0,kk-4);
    if(e0>=1) epi_q(O,e0,kk-2*e0-2);
    if(e1<=15) epi_q(O,e1,kk-2*e1-2);
  };
  #define TS(i)
  rsrc_t rwa = rw2, rwb = wrsrc(3);
  auto interval = [&](f32x16& N, f32x16& O, int u, auto ODDC){
    constexpr int ODD = decltype(ODDC)::v;
    TS(0);
    epi_load(u-1);
    if(fb==0){ if(u==w4+1) touch_lines(cpf, lbase + K1_DUM + wid*512, lane); }
    static_for<0,36>([&](auto KKC){
      constexpr int kk = decltype(KKC)::v;
      constexpr int kp = kk+PD;
      if(kp==20) wadv(5*8192);
      if(kp==36) wadv(ODD ? -14*8192 : 4*8192);
      pw[kp%(PD+1)] = ldW(kp<20 ? (kp>>2)*8192 : (kp<36 ? ((kp-20)>>2)*8192 : 0), kp&3);
      if(kk==1+2*fb) dma16i<(1-ODD)*K1_U+5*8192,5*WKT>(rwa,lbS,voS);
      if(kk==5+2*fb) dma16i<(1-ODD)*K1_U+6*8192,6*WKT>(rwa,lbS,voS);
      if(kk==9+2*fb) dma16i<(1-ODD)*K1_U+7*8192,7*WKT>(rwa,lbS,voS);
      if(kk==13+2*fb) dma16i<(1-ODD)*K1_U+8*8192,8*WKT>(rwa,lbS,voS);
      if(kk==20+fb) dma16i<ODD*K1_U+0*8192,0*WKT>(rwb,lbS,voS);
      if(kk==23+fb) dma16i<ODD*K1_U+1*8192,1*WKT>(rwb,lbS,voS);
      if(kk==26+fb) dma16i<ODD*K1_U+2*8192,2*WKT>(rwb,lbS,voS);
      if(kk==29+fb) dma16i<ODD*K1_U+3*8192,3*WKT>(rwb,lbS,voS);
      if(kk==32+fb) dma16i<ODD*K1_U+4*8192,4*WKT>(rwb,lbS,voS);
      SB();
      if(kk==0){ f32x16 z;
        #pragma unroll
        for(int i=0;i<16;i++) z[i]=0.f;
        N = __builtin_amdgcn_mfma_f32_32x32x16_bf16(pw[0], X[0], z, 0,0,0);
        cs = __builtin_amdgcn_mfma_f32_16x16x32_bf16(sel, pw[0], (f32x4){0.f,0.f,0.f,0.f}, 0,0,0); }
      else { N = __builtin_amdgcn_mfma_f32_32x32x16_bf16(pw[kk%(PD+1)], X[kk], N, 0,0,0);
        if((kk&3)==0) cs = __builtin_amdgcn_mfma_f32_16x16x32_bf16(sel, pw[kk%(PD+1)], cs, 0,0,0);
      }
      if(kk==2) epi_fix(O);
      epi_step(O, kk);
      if(kk==22+fb) epi_store();
      if(kk==2+fb){ if(u>1) epi_store(); }
      if(kk==34){ put_cs(ODD); rwa = rwb; rwb = wrsrc(u+3); }
      SB();
      if(kk==19){ TS(1); WAITV(4); BARRIER(); TS(2); if(fb==1){ if(u==w4+1) touch_lines(cpf, lbase + K1_DUM + wid*512, lane); } }
      if(kk==34 && !ODD){ if(u==4) WAITV(0); }
      if(kk==35){ TS(3); WAITV(5); asm volatile("s_waitcnt lgkmcnt(%0)"::"n"(AB ? 1 : 2):"memory"); BARRIER(); }
    });
    TS(10);
    STAMP(0,2+u);
  };
  #pragma unroll 1
  for(int it=0; it<3; it++){
    interval(Q, P, 2*it+1, IC<1>{});
    if(it<2) interval(P, Q, 2*it+2, IC<0>{});
  }
  epi_store();
  #pragma unroll
  for(int i=0;i<3;i++) sentinel_fill(slab, L*8 + wid + i*1920, lane);
  epi_load(5);
  epi_fix(Q);
  static_for<4,36>([&](auto KKC){ epi_step(Q,decltype(KKC)::v); if(decltype(KKC)::v==19 || decltype(KKC)::v==35) epi_store(); });
  STAMP(0,10);
}

__global__ __launch_bounds__(512,2) void k1_kernel(const u16* __restrict__ x, const u16* __restrict__ wre, const u16* __restrict__ wim,
    const u16* __restrict__ w1, const u16* __restrict__ b1, const u16* __restrict__ w2,
    char* himg, char* abimg, char* w2img, unsigned* cnt, unsigned long long* stamps, const u16* __restrict__ cr, const u16* __restrict__ ci){
  extern __shared__ __attribute__((aligned(16))) char smem[];
  const int bid = blockIdx.x; const int xcd = bid&7, idx = bid>>3;
  const int wid = __builtin_amdgcn_readfirstlane(threadIdx.x>>6), lane = threadIdx.x&63;
  STAMP(0,0);
  const u16* cpf = (bid<128 ? cr : ci) + (size_t)((bid&127)*512 + wid*64)*64;
  if(idx>=30){
    touch_lines(cpf, (unsigned)(size_t)(LAS char*)smem + wid*256, lane);
    const rsrc_t rs = mk_rsrc(w2), rd = mk_rsrc(w2img);
    const int r = lane&31, hh = lane>>5, gw = ((idx-30)*8 + xcd)*8 + wid;
    const unsigned stg = (unsigned)(size_t)(LAS char*)smem + 4096 + wid*16384;
    const unsigned vo0 = (lane>>3)*1152 + (((lane&7) ^ (lane>>4))*16);
    #pragma unroll 1
    for(int b=0;b<3;b++){
      #pragma unroll
      for(int i=0;i<4;i++){ int t = gw + (b*4+i)*128; if(t>1295) t = 1295; const int kb = t/9, cb = t - kb*9;
        #pragma unroll
        for(int p=0;p<4;p++) dma16(rs, stg + i*4096 + p*1024, (p&1) ? (vo0^64) : vo0, (unsigned)((kb*32 + p*8)*1152 + cb*128)); }
      WAITV(0);
      #pragma unroll
      for(int i=0;i<4;i++){ int t = gw + (b*4+i)*128; if(t>1295) t = 1295; const int kb = t/9, cb = t - kb*9;
        #pragma unroll
        for(int q=0;q<4;q++){
          u32x4 v = *(const LAS u32x4*)((LAS char*)(stg + i*4096 + r*128 + (((2*q+hh) ^ ((r>>1)&7))<<4)));
          const int ce = cb*8 + 2*q, nt = ce/18, cin = ce - nt*18;
          __builtin_amdgcn_raw_buffer_store_b128(v, rd, hh*512 + ((r ^ (4*hh))*16) + ((nt*144 + kb)*18 + cin)*512, 0, 16); } }
      WAITL0();
    }
    #pragma unroll
    for(int i=0;i<9;i++) sentinel_fill(cnt, 5760 + gw*9 + i, lane);
    STAMP(0,11);
    return; }
  const int L = xcd*30+idx; const int mh = L/120, rem = L - mh*120; const int nb = rem>>3, mb = mh*8 + (rem&7);
  if(nb<12){ if(wid<4) k1_body<false,0>(smem,x,wre,wim,w1,b1,w2,himg,abimg,w2img,mb,nb,L,wid,lane,stamps,cpf,cnt);
             else      k1_body<false,1>(smem,x,wre,wim,w1,b1,w2,himg,abimg,w2img,mb,nb,L,wid,lane,stamps,cpf,cnt); }
  else     { if(wid<4) k1_body<true ,0>(smem,x,wre,wim,w1,b1,w2,himg,abimg,w2img,mb,nb,L,wid,lane,stamps,cpf,cnt);
             else      k1_body<true ,1>(smem,x,wre,wim,w1,b1,w2,himg,abimg,w2img,mb,nb,L,wid,lane,stamps,cpf,cnt); }
}

constexpr int K2_AB = 128*128;
constexpr int K2_BB = 64*288;
constexpr int K2_ST = K2_AB + K2_BB;
constexpr int K2_LDS = K2_ST*4;

template<int NP_B>
DI void k2_loader(char* smem, const char* Abase, bool lin, const char* Bbase, int T, int lw, int lane){
  const unsigned lbase = (unsigned)(size_t)(LAS char*)smem;
  const rsrc_t rA = mk_rsrc(Abase), rB = mk_rsrc(Bbase);
  unsigned voA[4];
  #pragma unroll
  for(int p=0;p<4;p++){ int row = (lw*4+p)*8 + (lane>>3); int c = (lane&7) ^ ((row>>1)&7);
    voA[p] = lin ? (unsigned)((lw*4+p)*1024 + lane*16) : (unsigned)(row*(SEQ*2) + c*16); }
  const unsigned astep = lin ? 16384u : 128u;
  const int bp0 = lw<2 ? lw*5 : 10 + (lw-2)*4;
  const unsigned voB = bp0*1024 + lane*16;
  constexpr int P = 4+NP_B;
  auto issue = [&](int st){
    unsigned dst = lbase + (st&3)*K2_ST;
    #pragma unroll
    for(int p=0;p<4;p++) dma16(rA, dst + (lw*4+p)*1024, voA[p], st*astep);
    #pragma unroll
    for(int p=0;p<NP_B;p++) dma16(rB, dst + K2_AB + (bp0+p)*1024, voB, st*K2_BB + p*1024);
  };
  issue(0); issue(1); issue(2);
  WAITV(2*P);
  BARRIER();
  for(int t=0;t<T;t++){
    if(t+2<T) WAITV(P); else WAITV(0);
    BARRIER();
    if(t+3<T) issue(t+3);
  }
}

__global__ __launch_bounds__(512,2) void k2_kernel(const u16* __restrict__ x, const u16* __restrict__ cr, const u16* __restrict__ ci,
    const char* abimg, const char* himg, const char* w2img, const u16* __restrict__ b2,
    float* slab, unsigned* cnt, u16* out, unsigned long long* stamps){
  extern __shared__ __attribute__((aligned(16))) char smem[];
  const int bid = blockIdx.x; const int xcd = bid&7, idx = bid>>3;
  const int sp = xcd>>1;
  const int mt = (xcd&1)*8 + (idx>>2), nt = idx&3;
  const int tile = mt*4+nt;
  const int wid = __builtin_amdgcn_readfirstlane(threadIdx.x>>6), lane = threadIdx.x&63;
  const int T = sp<2 ? 36 : 32;
  f32x4 acc[4][9];
  STAMP(1,0);
  if(wid>=4){
    const char* Abase; const char* Bbase;
    if(sp<2){ Abase = himg + (size_t)(mt*72 + sp*36)*16384; Bbase = w2img + (size_t)(nt*144 + sp*72)*9216; }
    else { Abase = (const char*)((sp==2?cr:ci) + (size_t)mt*128*SEQ); Bbase = abimg + (size_t)(((sp-2)*4+nt)*64)*9216; }
    if(wid<6) k2_loader<5>(smem, Abase, sp<2, Bbase, T, wid-4, lane);
    else      k2_loader<4>(smem, Abase, sp<2, Bbase, T, wid-4, lane);
  } else {
    const int mi = wid>>1, ki = wid&1;
    const int fr = lane&15, g = lane>>4;
    int aoff0, aoff1;
    if(sp<2){ aoff0 = 2*mi*4096 + (4*ki+g)*512 + fr*16; aoff1 = aoff0 + 256; }
    else { aoff0 = (64*mi + fr)*128 + (((4*ki+g) ^ (fr>>1))*16); aoff1 = aoff0 + 2048; }
    const int q = fr>>2, p = lane&3, ph = p>>1;
    const int boff0 = K2_AB + ki*9216 + ph*512 + (8*g + 4*ph + q)*16 + (p&1)*8;
    const int boff1 = K2_AB + ki*9216 + ph*512 + (8*g + 4*(1-ph) + q)*16 + (p&1)*8;
    #pragma unroll
    for(int m=0;m<4;m++)
      #pragma unroll
      for(int n=0;n<9;n++) acc[m][n] = (f32x4){0.f,0.f,0.f,0.f};
    constexpr int BD = K2BD;
    bf16x8 A0[4], A1[4], B[9];
    auto ldA = [&](bf16x8 (&A)[4], int st){ const char* p0 = smem + (st&3)*K2_ST;
      A[0] = *(const bf16x8*)(p0 + aoff0); A[1] = *(const bf16x8*)(p0 + aoff1);
      A[2] = *(const bf16x8*)(p0 + aoff0 + 4096); A[3] = *(const bf16x8*)(p0 + aoff1 + 4096); };
    auto ldB = [&](int st, int n)->bf16x8 { char* p0 = smem + (st&3)*K2_ST + n*1024;
      s16x4 lo = __builtin_amdgcn_ds_read_tr16_b64_v4i16((lds_s16x4)(p0 + boff0));
      s16x4 hi = __builtin_amdgcn_ds_read_tr16_b64_v4i16((lds_s16x4)(p0 + boff1));
      return __builtin_shufflevector(lo,hi,0,1,2,3,4,5,6,7); };
    BARRIER();
    STAMP(1,1);
    ldA(A0,0);
    #pragma unroll
    for(int n=0;n<BD;n++) B[n]=ldB(0,n);
    s16x4 Bl[9], Bh[9];
    #pragma unroll
    for(int n=0;n<BD;n++){ Bl[n] = __builtin_shufflevector(B[n],B[n],0,1,2,3); Bh[n] = __builtin_shufflevector(B[n],B[n],4,5,6,7); }
    auto step = [&](bf16x8 (&Ac)[4], bf16x8 (&An)[4], int t){
      char* s0 = smem + (t&3)*K2_ST; char* s1 = smem + ((t+1)&3)*K2_ST;
      #pragma unroll
      for(int n=0;n<9;n++){
        if(n+BD==9) BARRIER();
        const int np = n+BD<9 ? n+BD : n+BD-9;
        char* pb = (n+BD<9 ? s0 : s1) + np*1024;
        const bf16x8 Bc = __builtin_shufflevector(Bl[n],Bh[n],0,1,2,3,4,5,6,7);
        #pragma unroll
        for(int m=0;m<4;m++){
          acc[m][n] = __builtin_amdgcn_mfma_f32_16x16x32_bf16(Bc, Ac[m], acc[m][n], 0,0,0);
          SB();
          if(m==0) Bl[np] = __builtin_amdgcn_ds_read_tr16_b64_v4i16((lds_s16x4)(pb + boff0));
          if(m==1) Bh[np] = __builtin_amdgcn_ds_read_tr16_b64_v4i16((lds_s16x4)(pb + boff1));
          if(m>=2 && n+BD>=9 && n+BD<=10){ const int a = (n+BD-9)*2 + (m-2); An[a] = *(const bf16x8*)(s1 + ((a&1)?aoff1:aoff0) + (a>>1)*4096); }
          SB();
        }
      }
    };
    for(int t=0;t<T;t+=2){ step(A0,A1,t); step(A1,A0,t+1); }
  }
  STAMP(1,2);
  BARRIER();
  if(wid<4){
    const int ki = wid&1;
    char* dst = smem + (wid^1)*18432 + lane*16;
    #pragma unroll
    for(int j=0;j<2;j++)
      #pragma unroll
      for(int n=0;n<9;n++){ f32x4 v = ki ? acc[j][n] : acc[2+j][n]; *(f32x4*)(dst + (j*9+n)*1024) = v; }
  }
  __syncthreads();
  STAMP(1,3);
  f32x4* own = (f32x4*)(smem + 4*18432);
  const rsrc_t rS = mk_rsrc(slab);
  if(wid<4){
    const int ki = wid&1;
    const char* src = smem + wid*18432 + lane*16;
    const unsigned so = (unsigned)(((tile*4 + wid)*4 + sp)*9216) + lane*16;
    #pragma unroll
    for(int n=0;n<9;n++){
      f32x4 v0 = (ki ? acc[2][n] : acc[0][n]) + *(const f32x4*)(src + n*1024);
      f32x4 v1 = (ki ? acc[3][n] : acc[1][n]) + *(const f32x4*)(src + (9+n)*1024);
      if(wid==sp){ own[n*64+lane] = v0; own[(9+n)*64+lane] = v1; }
      else { u32x4 w = {pk_bf16(v0[0],v0[1]), pk_bf16(v0[2],v0[3]), pk_bf16(v1[0],v1[1]), pk_bf16(v1[2],v1[3])};
        #pragma unroll
        for(int e=0;e<4;e++) w[e] = min(w[e], 0xfffffffeu);
        __builtin_amdgcn_raw_buffer_store_b128(w, rS, so + n*1024, 0, 16); }
    }
  }
  STAMP(1,4);
  const int fr = lane&15, g = lane>>4;
  const int nu = wid==4 ? 2 : 1;
  u32x2 xv[2][2], bv[2];
  #pragma unroll
  for(int i=0;i<2;i++){ if(i<nu){ const int n = i ? 8 : wid; const int col = nt*144 + 16*n + 4*g;
      #pragma unroll
      for(int j=0;j<2;j++) xv[i][j] = *(const u32x2*)(x + (size_t)(mt*128 + 32*sp + 16*j + fr)*DM + col);
      bv[i] = *(const u32x2*)(b2 + col); } }
  u32x4 pp[2][3];
  __syncthreads();
  { int spins = 0; unsigned done = 0;
    while(true){
      #pragma unroll
      for(int q=0;q<3;q++){ if(!((done>>q)&1)){ const int ss = q + (q>=sp ? 1 : 0);
        #pragma unroll
        for(int i=0;i<2;i++){ if(i<nu){ const int n = i ? 8 : wid;
          pp[i][q] = __builtin_amdgcn_raw_buffer_load_b128(rS, (unsigned)(((tile*4 + sp)*4 + ss)*9216) + n*1024 + lane*16, 0, 16); } } } }
      #pragma unroll
      for(int q=0;q<3;q++){ if(!((done>>q)&1)){ unsigned all = 0;
        #pragma unroll
        for(int i=0;i<2;i++){ if(i<nu) all = max(all, max(max(pp[i][q][0],pp[i][q][1]), max(pp[i][q][2],pp[i][q][3]))); }
        if(__all(all != 0xffffffffu)) done |= 1u<<q; } }
      if(done==7u) break;
      if(++spins > (1<<20)) break;
      __builtin_amdgcn_s_sleep(PSLEEP);
    } }
  STAMP(1,5);
  #pragma unroll
  for(int i=0;i<2;i++){ if(i<nu){ const int n = i ? 8 : wid; const int col = nt*144 + 16*n + 4*g;
    const float bf[4] = {__uint_as_float(bv[i].x<<16), __uint_as_float(bv[i].x&0xffff0000u), __uint_as_float(bv[i].y<<16), __uint_as_float(bv[i].y&0xffff0000u)};
    #pragma unroll
    for(int j=0;j<2;j++){
      const f32x4 ow = own[(j*9+n)*64+lane];
      f32x4 p[3];
      #pragma unroll
      for(int q=0;q<3;q++){ const unsigned a = pp[i][q][2*j], b = pp[i][q][2*j+1];
        p[q] = (f32x4){__uint_as_float(a<<16), __uint_as_float(a&0xffff0000u), __uint_as_float(b<<16), __uint_as_float(b&0xffff0000u)}; }
      f32x4 mm, sv;
      if(sp<2){ mm = ow + p[0]; sv = p[1] + p[2]; }
      else    { mm = p[0] + p[1]; sv = ow + p[2]; }
      const float xf[4] = {__uint_as_float(xv[i][j].x<<16), __uint_as_float(xv[i][j].x&0xffff0000u), __uint_as_float(xv[i][j].y<<16), __uint_as_float(xv[i][j].y&0xffff0000u)};
      float o[4];
      #pragma unroll
      for(int e=0;e<4;e++){ float m = mm[e]+bf[e];
        float gt = m * __builtin_amdgcn_rcpf(1.f + __builtin_amdgcn_exp2f(-1.44269504f*m));
        o[e] = xf[e] + sv[e]*gt; }
      *(u32x2*)(out + (size_t)(mt*128 + 32*sp + 16*j + fr)*DM + col) = (u32x2){pk_bf16(o[0],o[1]), pk_bf16(o[2],o[3])};
    } } }
  STAMP(1,6);
}

extern "C" void kernel_launch(void* const* d_in, const int* in_sizes, int n_in,
                              void* d_out, int out_size, void* d_ws, size_t ws_size,
                              hipStream_t stream) {
  const u16* x=(const u16*)d_in[0]; const u16* cr=(const u16*)d_in[1]; const u16* ci=(const u16*)d_in[2];
  const u16* wre=(const u16*)d_in[3]; const u16* wim=(const u16*)d_in[4];
  const u16* w1=(const u16*)d_in[6]; const u16* b1=(const u16*)d_in[7]; const u16* w2=(const u16*)d_in[8]; const u16* b2=(const u16*)d_in[9];
  char* ws=(char*)d_ws;
  char* himg = ws; char* abimg = ws + (20<<20); char* w2img = ws + (26<<20);
  float* slab=(float*)(ws+(32<<20)); unsigned* cnt=(unsigned*)(ws+(52<<20));
  unsigned long long* stamps = (unsigned long long*)(ws+(128<<20));
  (void)hipFuncSetAttribute((const void*)k1_kernel, hipFuncAttributeMaxDynamicSharedMemorySize, K1_LDS);
  (void)hipFuncSetAttribute((const void*)k2_kernel, hipFuncAttributeMaxDynamicSharedMemorySize, K2_LDS);
  k1_kernel<<<256,512,K1_LDS,stream>>>(x,wre,wim,w1,b1,w2,himg,abimg,w2img,(unsigned*)slab,stamps,cr,ci);
  k2_kernel<<<256,512,K2_LDS,stream>>>(x,cr,ci,abimg,himg,w2img,b2,slab,cnt,(u16*)d_out,stamps);
}
```

```cpp
#include <hip/hip_runtime.h>
#include <hip/hip_bf16.h>
typedef unsigned short u16;
typedef short bf16x8 __attribute__((ext_vector_type(8)));
typedef short s16x4 __attribute__((ext_vector_type(4)));
typedef float f32x4 __attribute__((ext_vector_type(4)));
typedef float f32x2 __attribute__((ext_vector_type(2)));
typedef float f32x16 __attribute__((ext_vector_type(16)));
typedef unsigned u32x4 __attribute__((ext_vector_type(4)));
typedef unsigned u32x2 __attribute__((ext_vector_type(2)));
typedef __bf16 bf2 __attribute__((ext_vector_type(2)));
#define LAS __attribute__((address_space(3)))
typedef LAS s16x4* lds_s16x4;
typedef LAS void* lds_vp;
typedef __amdgpu_buffer_rsrc_t rsrc_t;
#define DI __device__ __forceinline__
DI float bf2f(u16 v){ return __uint_as_float(((unsigned)v)<<16); }
DI u16 f2bf(float f){ unsigned u=__float_as_uint(f); u += 0x7fffu + ((u>>16)&1u); return (u16)(u>>16); }
DI unsigned pk_bf16(float lo, float hi){ bf2 v; v[0]=(__bf16)lo; v[1]=(__bf16)hi; return __builtin_bit_cast(unsigned,v); }
DI float opq(float x){ asm("" : "+v"(x)); return x; }
DI rsrc_t mk_rsrc(const void* p){ return __builtin_amdgcn_make_buffer_rsrc((void*)p, 0, 0x7fffffff, 0x00020000); }
#define ROT 0
#define PRIO 1
#define K2BD 5
#define CPF 1
#define PSLEEP 2
#define DEEP 0
#define ROTF ((mb + 3*nb + (nb/3)) % 9)
#define K2WM 12
#define K2WS 8
#define K2RHO 0
#define K2MIX 0
#define PFD 3
constexpr int SEQ=2048, DM=576, FF=4608;

#define STAMP(K,i)
#define SB() __builtin_amdgcn_sched_barrier(0)
#define BARRIER() do{ SB(); asm volatile("":::"memory"); __builtin_amdgcn_s_barrier(); asm volatile("":::"memory"); SB(); }while(0)
#define WAITV(n) asm volatile("s_waitcnt vmcnt(%0)"::"n"(n):"memory")
#define WAITL0() asm volatile("s_waitcnt lgkmcnt(0)":::"memory")

DI void dma16(rsrc_t r, unsigned lds_addr, unsigned voff, unsigned soff){
  asm volatile("s_mov_b32 m0, %0\n\ts_nop 4\n\tbuffer_load_dwordx4 %1, %2, %3 offen lds"
               :: "s"(lds_addr), "v"(voff), "s"(r), "s"(soff) : "memory");
}

template<int LOFF, int SOFF>
DI void dma16i(rsrc_t r, unsigned lds_base, unsigned voff){
  unsigned tmp;
  asm volatile("s_add_u32 m0, %1, %2\n\ts_mov_b32 %0, %3\n\ts_nop 3\n\tbuffer_load_dwordx4 %4, %5, %0 offen lds"
               : "=&s"(tmp) : "s"(lds_base), "i"(LOFF), "i"(SOFF), "v"(voff), "s"(r) : "memory", "scc");
}
template<int N> struct IC { static constexpr int v = N; };
template<int B, int E, class Fn> DI void static_for(Fn&& f){ if constexpr (B<E){ f(IC<B>{}); static_for<B+1,E>(f); } }
constexpr int K1_U = 73728;
constexpr int K1_STAT = 2*K1_U;
constexpr int K1_B1   = K1_STAT + 2048;
constexpr int K1_CSP  = K1_B1 + 1536;
constexpr int K1_DUM  = K1_CSP + 2048;
constexpr int K1_LDS  = K1_DUM + 4096;

template<int LOFF>
DI void dma16f(rsrc_t r, unsigned lds_base, unsigned voff, unsigned soff){
  asm volatile("s_add_u32 m0, %0, %1\n\ts_nop 3\n\tbuffer_load_dwordx4 %2, %3, %4 offen lds"
               :: "s"(lds_base), "i"(LOFF), "v"(voff), "s"(r), "s"(soff) : "memory", "scc");
}
DI void sentinel_fill(const void* slab, int p, int lane){
  const int tq = p/27, rem = p - tq*27, k3 = rem/9, n = rem - k3*9, q = tq&3, src = k3 + (k3>=q ? 1 : 0);
  const u32x4 ff = {0xffffffffu,0xffffffffu,0xffffffffu,0xffffffffu};
  __builtin_amdgcn_raw_buffer_store_b128(ff, mk_rsrc(slab), (unsigned)((tq*4 + src)*9216 + n*1024) + lane*16, 0, 16);
}
DI void touch_lines(const u16* p, unsigned lds_dummy, int lane){
  const rsrc_t rb = mk_rsrc(p);
  asm volatile("s_mov_b32 m0, %0\n\ts_nop 4\n\tbuffer_load_dword %1, %2, 0 offen lds" :: "s"(lds_dummy), "v"(lane*128), "s"(rb) : "memory");
}
template<bool AB, int fb>
DI void k1_body(char* smem, const u16* x, const u16* wre, const u16* wim, const u16* w1, const u16* b1, const u16* w2,
                char* himg, char* abimg, char* w2img, int mb, int nb, int L, int wid, int lane, unsigned long long* stamps, const u16* cpf, const void* slab){
  const unsigned lbase = (unsigned)(size_t)(LAS char*)smem;
  const int w4 = wid&3;
  const int r = lane&31, hh = lane>>5;
  constexpr int WROW = AB ? 1152 : FF*2;
  constexpr int WKT  = AB ? 128 : 64*FF*2;
  const rsrc_t rX = mk_rsrc(x + (size_t)mb*128*DM);
  const unsigned voR0 = (lane>>3)*1152 + (((lane&7) ^ (lane>>4))*16);
  const unsigned voW0 = AB ? voR0 : (unsigned)((lane>>3)*(FF*2) + (((lane&7) ^ (((lane>>4)&1)<<2))*16));
  auto wrsrc = [&](int u)->rsrc_t {
    const int uc = u<=5 ? u : 5; const u16* p;
    if constexpr (AB){ int ec = (nb-12)*384 + uc*64; p = ec>=576 ? wim + (size_t)(ec-576)*DM : wre + (size_t)ec*DM; }
    else p = w1 + nb*384 + uc*64;
    return __builtin_amdgcn_make_buffer_rsrc((void*)p, 0, u<=5 ? 0x7fffffff : 0, 0x00020000);
  };
  if constexpr (!AB && fb==0){ if(wid<3){
    const rsrc_t rb = mk_rsrc(b1 + nb*384 + wid*128);
    asm volatile("s_mov_b32 m0, %0\n\ts_nop 4\n\tbuffer_load_dword %1, %2, 0 offen lds" :: "s"(lbase + K1_B1 + wid*256), "v"(lane*4), "s"(rb) : "memory"); } }
  const unsigned voXa = voR0 + wid*16*1152, voXb = (voR0^64) + (wid*16+8)*1152;
  const unsigned voS = (AB ? (voW0 ^ ((wid&1)<<6)) : voW0) + wid*8*WROW;
  const unsigned lbX = lbase + wid*2048, lbS = lbase + wid*1024;
  unsigned kto[9];
  { const int rho = ROTF;
    #pragma unroll
    for(int i=0;i<9;i++){ int t = i + rho; if(t>=9) t -= 9; kto[i] = t; } }
  #define DMAX(kt) do{ dma16f<((3*(kt))%18)*8192>(rX, lbX, voXa, kto[kt]*128); dma16f<((3*(kt))%18)*8192+1024>(rX, lbX, voXb, kto[kt]*128); }while(0)
  #define DMAW(rw,slot,kt) dma16f<(slot)*8192>(rw, lbS, voS, kto[kt]*WKT)
  const rsrc_t rw0 = wrsrc(0), rw1 = wrsrc(1), rw2 = wrsrc(2);
  DMAX(0); DMAW(rw0,2,0); DMAX(1); DMAW(rw0,5,1); DMAX(2); DMAW(rw0,8,2);

  unsigned xoff[4];
  unsigned woff[4];
  #pragma unroll
  for(int sp=0;sp<4;sp++){
    int s = sp ^ w4;
    int row = 32*w4 + r;
    xoff[sp] = lbase + row*128 + (((2*s+hh) ^ ((row>>1)&7))<<4);
    if constexpr (AB){ int wr = 32*fb + r; woff[sp] = wr*128 + (((2*s+hh) ^ ((wr>>1)&7))<<4); }
    else { int q=(lane&15)>>2, p=lane&3, blk=(lane>>4)&1; int wr = 16*s + 8*hh + q;
           woff[sp] = wr*128 + ((64*fb + 32*blk + 8*p) ^ (((q>>1)&1)<<6)); }
    woff[sp] += lbase;
  }
  auto wadv = [&](int d){
    #pragma unroll
    for(int sp=0;sp<4;sp++){ woff[sp] += d; asm volatile("" : "+v"(woff[sp])); } };
  auto ldW = [&](int off, int sp)->bf16x8 {
    LAS char* p = (LAS char*)(woff[sp]) + off;
    if constexpr (AB){ return *(const LAS bf16x8*)p; }
    else { s16x4 lo = __builtin_amdgcn_ds_read_tr16_b64_v4i16((lds_s16x4)p);
           s16x4 hi = __builtin_amdgcn_ds_read_tr16_b64_v4i16((lds_s16x4)(p + 512));
           return __builtin_shufflevector(lo,hi,0,1,2,3,4,5,6,7); }
  };
  const short selv = ((lane&15) == ((lane>>4)&1)) ? (short)0x3f80 : (short)0;
  const bf16x8 sel = {selv,selv,selv,selv,selv,selv,selv,selv};

  bf16x8 X[36];
  f32x16 P, Q;
  f32x4 cs = {0.f,0.f,0.f,0.f}, sx = {0.f,0.f,0.f,0.f};
  #pragma unroll
  for(int i=0;i<16;i++){ P[i]=0.f; Q[i]=0.f; }
  constexpr int PD = PFD;
  bf16x8 pw[PD+1];
  STAMP(0,1);
  #define TW(i)
  TW(0);
  if constexpr (fb==1) __builtin_amdgcn_s_setprio(1);
  bf16x8 wf[2][4];
  auto smma = [&](auto KT, auto RDC){
    constexpr int kt = decltype(KT)::v; constexpr bool RD = decltype(RDC)::v;
    #pragma unroll
    for(int sp=0;sp<4;sp++){
      P = __builtin_amdgcn_mfma_f32_32x32x16_bf16(wf[kt&1][sp], X[kt*4+sp], P, 0,0,0);
      if constexpr (RD){ SB(); X[(kt+1)*4+sp] = *(const LAS bf16x8*)((LAS char*)(xoff[sp]) + (((kt+1)&1) ? 24576 : 0)); wf[(kt+1)&1][sp] = ldW(16384,sp); SB(); }
      if(sp==0) cs = __builtin_amdgcn_mfma_f32_16x16x32_bf16(sel, wf[kt&1][0], cs, 0,0,0);
      if((sp>>1)==fb){ Q = __builtin_amdgcn_mfma_f32_32x32x16_bf16(X[kt*4+sp], X[kt*4+sp], Q, 0,0,0);
                       sx = __builtin_amdgcn_mfma_f32_16x16x32_bf16(sel, X[kt*4+sp], sx, 0,0,0); }
    }
    asm volatile("" : "+v"(Q));
    asm volatile("" : "+v"(P), "+v"(sx), "+v"(cs));
  };
  auto sstep = [&](auto KT){
    constexpr int kt = decltype(KT)::v;
    WAITV(6);
    WAITL0();
    BARRIER();
    TW(1+kt);
    auto lds = [&](){
      #pragma unroll
      for(int sp=0;sp<4;sp++){ X[kt*4+sp] = *(const LAS bf16x8*)((LAS char*)(xoff[sp]) + ((kt&1) ? 24576 : 0)); wf[kt&1][sp] = ldW(16384,sp); }
    };
    auto dmas = [&](){
      if constexpr (kt<=5){ DMAX(kt+3); DMAW(rw0,(3*(kt+3)+2)%18,kt+3); }
      else { DMAW(rw1,9+3*(kt-6),3*(kt-6)); DMAW(rw1,10+3*(kt-6),3*(kt-6)+1); DMAW(rw1,11+3*(kt-6),3*(kt-6)+2); }
    };
    if constexpr (kt==0){ lds(); SB(); dmas(); }
    else if constexpr (fb==0){ dmas(); SB(); lds(); SB(); smma(IC<kt-1>{}, IC<0>{}); }
    else     { smma(IC<kt-1>{}, IC<1>{}); SB(); dmas(); }
    wadv(kt==5 ? -5*24576 : 24576);
    if(kt&1){
      #pragma unroll
      for(int sp=0;sp<4;sp++){ xoff[sp] += (kt==5 ? -4*24576 : 2*24576); asm volatile("" : "+v"(xoff[sp])); } }
  };
  sstep(IC<0>{}); sstep(IC<1>{}); sstep(IC<2>{}); sstep(IC<3>{}); sstep(IC<4>{}); sstep(IC<5>{}); sstep(IC<6>{}); sstep(IC<7>{}); sstep(IC<8>{});
  smma(IC<8>{}, IC<0>{});
  if constexpr (fb==1) __builtin_amdgcn_s_setprio(0);
  auto put_cs = [&](int par){
    if(lane<16){ float* d = (float*)(smem + K1_CSP + par*1024 + wid*128 + lane*4); d[0] = cs[0]; d[16] = cs[1]; }
  };
  put_cs(0);
  if(lane<16){ float* d = (float*)(smem + K1_STAT + fb*512 + (32*w4+lane)*4); d[0] = sx[0]; d[16] = sx[1]; }
  { const int dr = (r&3) + 4*(r>>3); float dg = Q[0];
    #pragma unroll
    for(int i=1;i<16;i++) dg = (dr==i) ? Q[i] : dg;
    if(hh == ((r>>2)&1)) *(float*)(smem + K1_STAT + 1024 + fb*512 + (32*w4+r)*4) = dg; }
  WAITV(0); WAITL0();
  BARRIER();
  STAMP(0,2);
  #pragma unroll
  for(int i=0;i<PD;i++) pw[i] = ldW(0,i);
  float rr, irr, K0, K1c; bf16x8 Bext;
  { const float* sp_ = (const float*)(smem + K1_STAT + (32*w4+r)*4); const float s1 = sp_[0] + sp_[128], s2 = sp_[256] + sp_[384];
    float mu = s1*(1.f/DM); float var = fmaxf(s2*(1.f/DM) - mu*mu, 0.f);
    float sd = __builtin_amdgcn_sqrtf(var + 1e-5f);
    irr = sd;
    if constexpr (!AB){ float v2 = var*__builtin_amdgcn_rcpf(var + 1e-5f); irr = sd*__builtin_amdgcn_sqrtf(v2 + 1e-5f); }
    rr = __builtin_amdgcn_rcpf(irr);
    K0 = -2.30220820f*rr; K1c = -0.10294324f*rr*rr*rr;
    unsigned i0 = pk_bf16(irr,irr) & 0xffff0000u; unsigned m0 = pk_bf16(-mu,-mu) & 0xffff0000u;
    unsigned wi = pk_bf16(irr, irr - __uint_as_float(i0)), wm = pk_bf16(-mu, -mu - __uint_as_float(m0));
    u32x4 bx = {wi, wm, wm, 0u};
    if(hh) bx = (u32x4){0u,0u,0u,0u};
    Bext = __builtin_bit_cast(bf16x8, bx); }
  const rsrc_t rH = mk_rsrc(AB ? abimg : himg);

  float cp4[4]; unsigned bw = 0; float to[2]; u32x2 pk[2];
  unsigned obase; float a_r = rr; int ce0=0, ri=0;
  auto epi_load = [&](int u){
    if constexpr (AB){ int ec = (nb-12)*384 + u*64; ri = ec>=576; ce0 = ((ec - ri*576)>>3) + 4*fb; a_r = ri ? -rr : rr; obase = hh*512 + ((r ^ (4*hh))*16); }
    else { obase = (unsigned)(((mb*72 + nb*6 + u)*4 + w4)*4096 + fb*2048) + lane*16;
           bw = *(const u16*)(smem + K1_B1 + u*128 + fb*64 + r*2); }
    const float* cp = (const float*)(smem + K1_CSP + (u&1)*1024 + fb*512);
    cp4[0] = cp[r]; cp4[1] = cp[32+r]; cp4[2] = cp[64+r]; cp4[3] = cp[96+r];
  };
  auto epi_fix = [&](f32x16& O){
    float c = opq(opq(cp4[0]+cp4[1]) + opq(cp4[2]+cp4[3]));
    unsigned c0 = pk_bf16(c,c); float c1 = c - __uint_as_float(c0 & 0xffff0000u);
    u32x4 ax = {bw*0x10001u, c0, pk_bf16(c1,c1), 0u};
    O = __builtin_amdgcn_mfma_f32_32x32x16_bf16(__builtin_bit_cast(bf16x8,ax), Bext, O, 0,0,0);
  };
  float tq[3]; u32x4 vst; unsigned ost;
  auto epi_q = [&](const f32x16& O, int e, int stg){
    float& t = tq[e%3];
    if constexpr (AB){
      if(stg==3) to[e&1] = opq(a_r*O[e]);
    } else {
      const float v = O[e];
      if(stg==0) t = opq(fmaf(opq(v*v), K1c, K0));
      if(stg==1) t = opq(__builtin_amdgcn_exp2f(opq(v*t)));
      if(stg==2) t = opq(__builtin_amdgcn_rcpf(opq(fmaf(t, irr, irr))));
      if(stg==3) to[e&1] = opq(v*t);
    }
    if(stg==3){
      if(e&1) pk[(e>>2)&1][(e>>1)&1] = pk_bf16(to[0],to[1]);
      if((e&7)==7){
        auto s0 = __builtin_amdgcn_permlane32_swap(pk[0][0], pk[1][0], false, false);
        auto s1 = __builtin_amdgcn_permlane32_swap(pk[0][1], pk[1][1], false, false);
        u32x4 v = {s0[0], s1[0], s0[1], s1[1]};
        unsigned so;
        if constexpr (AB){ int ce = ce0 + (e>>3)*2; int nt = ce/18, cin = ce - nt*18; so = (unsigned)((((ri*4+nt)*64 + mb*4 + w4)*18 + cin)*512); }
        else so = (e>>3)*1024;
        vst = v; ost = obase + so;
      }
    }
  };
  auto epi_store = [&](){ __builtin_amdgcn_raw_buffer_store_b128(vst, rH, ost, 0, 16); };
  auto epi_step = [&](const f32x16& O, int kk){
    if(kk<4) return;
    const int e1 = (kk-2)>>1, e0 = e1-1;
    if(kk<8) epi_q(O,0,kk-4);
    if(e0>=1) epi_q(O,e0,kk-2*e0-2);
    if(e1<=15) epi_q(O,e1,kk-2*e1-2);
  };
  #define TS(i)
  rsrc_t rwa = rw2, rwb = wrsrc(3);
  auto interval = [&](f32x16& N, f32x16& O, int u, auto ODDC){
    constexpr int ODD = decltype(ODDC)::v & 1; constexpr bool FIRST = decltype(ODDC)::v == 3;
    TS(0);
    epi_load(u-1);
    if(fb==0){ if(u==w4+1) touch_lines(cpf, lbase + K1_DUM + wid*512, lane); }
    static_for<0,36>([&](auto KKC){
      constexpr int kk = decltype(KKC)::v;
      constexpr int kp = kk+PD;
      if(kp==20) wadv(5*8192);
      if(kp==36) wadv(ODD ? -14*8192 : 4*8192);
      pw[kp%(PD+1)] = ldW(kp<20 ? (kp>>2)*8192 : (kp<36 ? ((kp-20)>>2)*8192 : 0), kp&3);
      if constexpr (FIRST){
        if(kk==0+fb) dma16f<0*8192>(rwa,lbS,voS,kto[0]*WKT);
        if(kk==2+fb) dma16f<1*8192>(rwa,lbS,voS,kto[1]*WKT);
        if(kk==4+fb) dma16f<2*8192>(rwa,lbS,voS,kto[2]*WKT);
        if(kk==6+fb) dma16f<3*8192>(rwa,lbS,voS,kto[3]*WKT);
        if(kk==8+fb) dma16f<4*8192>(rwa,lbS,voS,kto[4]*WKT);
        if(kk==10+fb) dma16f<5*8192>(rwa,lbS,voS,kto[5]*WKT);
        if(kk==12+fb) dma16f<6*8192>(rwa,lbS,voS,kto[6]*WKT);
        if(kk==14+fb) dma16f<7*8192>(rwa,lbS,voS,kto[7]*WKT);
        if(kk==16+fb) dma16f<8*8192>(rwa,lbS,voS,kto[8]*WKT);
      } else {
      if(kk==1+2*fb) dma16f<(1-ODD)*K1_U+5*8192>(rwa,lbS,voS,kto[5]*WKT);
      if(kk==5+2*fb) dma16f<(1-ODD)*K1_U+6*8192>(rwa,lbS,voS,kto[6]*WKT);
      if(kk==9+2*fb) dma16f<(1-ODD)*K1_U+7*8192>(rwa,lbS,voS,kto[7]*WKT);
      if(kk==13+2*fb) dma16f<(1-ODD)*K1_U+8*8192>(rwa,lbS,voS,kto[8]*WKT);
      }
      if(kk==21+fb ) dma16f<ODD*K1_U+0*8192>(rwb,lbS,voS,kto[0]*WKT);
      if(kk==24+fb ) dma16f<ODD*K1_U+1*8192>(rwb,lbS,voS,kto[1]*WKT);
      if(kk==27+fb ) dma16f<ODD*K1_U+2*8192>(rwb,lbS,voS,kto[2]*WKT);
      if(kk==30+fb ) dma16f<ODD*K1_U+3*8192>(rwb,lbS,voS,kto[3]*WKT);
      if(kk==32+fb ) dma16f<ODD*K1_U+4*8192>(rwb,lbS,voS,kto[4]*WKT);
      SB();
      if(kk==0){ f32x16 z;
        #pragma unroll
        for(int i=0;i<16;i++) z[i]=0.f;
        N = __builtin_amdgcn_mfma_f32_32x32x16_bf16(pw[0], X[0], z, 0,0,0);
        cs = __builtin_amdgcn_mfma_f32_16x16x32_bf16(sel, pw[0], (f32x4){0.f,0.f,0.f,0.f}, 0,0,0); }
      else { N = __builtin_amdgcn_mfma_f32_32x32x16_bf16(pw[kk%(PD+1)], X[kk], N, 0,0,0);
        if((kk&3)==0) cs = __builtin_amdgcn_mfma_f32_16x16x32_bf16(sel, pw[kk%(PD+1)], cs, 0,0,0);
      }
      if(kk==2) epi_fix(O);
      epi_step(O, kk);
      if(kk==20) epi_store();
      if(kk==0 && !FIRST) epi_store();
      if(kk==34){ put_cs(ODD); rwa = rwb; rwb = wrsrc(u+3); }
      SB();
      if(kk==19){ TS(1); if constexpr (FIRST) WAITV(8); else WAITV(4); BARRIER(); TS(2); if(fb==1){ if(u==(w4<3 ? w4+1 : 3)) touch_lines(cpf, lbase + K1_DUM + wid*512, lane); } }
      if(kk==34 && !ODD){ if(u==4) WAITV(0); }
      if(kk==35){ TS(3); WAITV(5); asm volatile("s_waitcnt lgkmcnt(%0)"::"n"(AB ? 1 : 2):"memory"); BARRIER(); }
    });
    TS(10);
    STAMP(0,2+u);
  };
  interval(Q, P, 1, IC<3>{});
  #pragma unroll 1
  for(int it=1; it<3; it++){
    interval(P, Q, 2*it, IC<0>{});
    interval(Q, P, 2*it+1, IC<1>{});
  }
  epi_store();
  #pragma unroll
  for(int i=0;i<3;i++) sentinel_fill(slab, L*8 + wid + i*1920, lane);
  epi_load(5);
  epi_fix(Q);
  static_for<4,36>([&](auto KKC){ epi_step(Q,decltype(KKC)::v); if(decltype(KKC)::v==19 || decltype(KKC)::v==35) epi_store(); });
  STAMP(0,10);
}

__global__ __launch_bounds__(512,2) void k1_kernel(const u16* __restrict__ x, const u16* __restrict__ wre, const u16* __restrict__ wim,
    const u16* __restrict__ w1, const u16* __restrict__ b1, const u16* __restrict__ w2,
    char* himg, char* abimg, char* w2img, unsigned* cnt, unsigned long long* stamps, const u16* __restrict__ cr, const u16* __restrict__ ci){
  extern __shared__ __attribute__((aligned(16))) char smem[];
  const int bid = blockIdx.x; const int xcd = bid&7, idx = bid>>3;
  const int wid = __builtin_amdgcn_readfirstlane(threadIdx.x>>6), lane = threadIdx.x&63;
  STAMP(0,0);
  const u16* cpf = (bid<128 ? cr : ci) + (size_t)((bid&127)*512 + wid*64)*64;
  if(idx>=30){
    touch_lines(cpf, (unsigned)(size_t)(LAS char*)smem + wid*256, lane);
    const rsrc_t rs = mk_rsrc(w2), rd = mk_rsrc(w2img);
    const int r = lane&31, hh = lane>>5, gw = ((idx-30)*8 + xcd)*8 + wid;
    const unsigned stg = (unsigned)(size_t)(LAS char*)smem + 4096 + wid*16384;
    const unsigned vo0 = (lane>>3)*1152 + (((lane&7) ^ (lane>>4))*16);
    #pragma unroll 1
    for(int b=0;b<3;b++){
      #pragma unroll
      for(int i=0;i<4;i++){ int t = gw + (b*4+i)*128; if(t>1295) t = 1295; const int kb = t/9, cb = t - kb*9;
        #pragma unroll
        for(int p=0;p<4;p++) dma16(rs, stg + i*4096 + p*1024, (p&1) ? (vo0^64) : vo0, (unsigned)((kb*32 + p*8)*1152 + cb*128)); }
      WAITV(0);
      #pragma unroll
      for(int i=0;i<4;i++){ int t = gw + (b*4+i)*128; if(t>1295) t = 1295; const int kb = t/9, cb = t - kb*9;
        #pragma unroll
        for(int q=0;q<4;q++){
          u32x4 v = *(const LAS u32x4*)((LAS char*)(stg + i*4096 + r*128 + (((2*q+hh) ^ ((r>>1)&7))<<4)));
          const int ce = cb*8 + 2*q, nt = ce/18, cin = ce - nt*18;
          __builtin_amdgcn_raw_buffer_store_b128(v, rd, hh*512 + ((r ^ (4*hh))*16) + ((nt*144 + kb)*18 + cin)*512, 0, 16); } }
      WAITL0();
    }
    #pragma unroll
    for(int i=0;i<9;i++) sentinel_fill(cnt, 5760 + gw*9 + i, lane);
    STAMP(0,11);
    return; }
  const int L = xcd*30+idx; const int mh = L/120, rem = L - mh*120; const int nb = rem>>3, mb = mh*8 + (rem&7);
  if(nb<12){ if(wid<4) k1_body<false,0>(smem,x,wre,wim,w1,b1,w2,himg,abimg,w2img,mb,nb,L,wid,lane,stamps,cpf,cnt);
             else      k1_body<false,1>(smem,x,wre,wim,w1,b1,w2,himg,abimg,w2img,mb,nb,L,wid,lane,stamps,cpf,cnt); }
  else     { if(wid<4) k1_body<true ,0>(smem,x,wre,wim,w1,b1,w2,himg,abimg,w2img,mb,nb,L,wid,lane,stamps,cpf,cnt);
             else      k1_body<true ,1>(smem,x,wre,wim,w1,b1,w2,himg,abimg,w2img,mb,nb,L,wid,lane,stamps,cpf,cnt); }
}

constexpr int K2_AB = 128*128;
constexpr int K2_BB = 64*288;
constexpr int K2_ST = K2_AB + K2_BB;
constexpr int K2_LDS = K2_ST*4;

template<int NP_B>
DI void k2_loader(char* smem, const char* Abase, bool lin, const char* Bbase, int T, int lw, int lane, int W, int rho){
  const unsigned lbase = (unsigned)(size_t)(LAS char*)smem;
  const rsrc_t rA = mk_rsrc(Abase), rB = mk_rsrc(Bbase);
  unsigned voA[4];
  #pragma unroll
  for(int p=0;p<4;p++){ int row = (lw*4+p)*8 + (lane>>3); int c = (lane&7) ^ ((row>>1)&7);
    voA[p] = lin ? (unsigned)((lw*4+p)*1024 + lane*16) : (unsigned)(row*(SEQ*2) + c*16); }
  const unsigned astep = lin ? 16384u : 128u;
  const int bp0 = lw<2 ? lw*5 : 10 + (lw-2)*4;
  const unsigned voB = bp0*1024 + lane*16;
  constexpr int P = 4+NP_B;
  int wbase = 0, wpos = rho, wcnt = 0;
  auto issue = [&](int st){
    const int ks = wbase + wpos;
    if(++wpos==W) wpos = 0;
    if(++wcnt==W){ wcnt = 0; wbase += W; }
    unsigned dst = lbase + (st&3)*K2_ST;
    #pragma unroll
    for(int p=0;p<4;p++) dma16(rA, dst + (lw*4+p)*1024, voA[p], ks*astep);
    #pragma unroll
    for(int p=0;p<NP_B;p++) dma16(rB, dst + K2_AB + (bp0+p)*1024, voB, ks*K2_BB + p*1024);
  };
  issue(0); issue(1); issue(2);
  WAITV(2*P);
  BARRIER();
  for(int t=0;t<T;t++){
    if(t+2<T) WAITV(P); else WAITV(0);
    BARRIER();
    if(t+3<T) issue(t+3);
  }
}

__global__ __launch_bounds__(512,2) void k2_kernel(const u16* __restrict__ x, const u16* __restrict__ cr, const u16* __restrict__ ci,
    const char* abimg, const char* himg, const char* w2img, const u16* __restrict__ b2,
    float* slab, unsigned* cnt, u16* out, unsigned long long* stamps){
  extern __shared__ __attribute__((aligned(16))) char smem[];
  const int bid = blockIdx.x; const int xcd = bid&7, idx = bid>>3;
  const int sp = xcd>>1;
  const int mt = (xcd&1)*8 + (idx>>2), nt = idx&3;
  const int tile = mt*4+nt;
  const int wid = __builtin_amdgcn_readfirstlane(threadIdx.x>>6), lane = threadIdx.x&63;
  const int T = sp<2 ? 36 : 32;
  f32x4 acc[4][9];
  STAMP(1,0);
  if(wid>=4){
    const char* Abase; const char* Bbase;
    if(sp<2){ Abase = himg + (size_t)(mt*72 + sp*36)*16384; Bbase = w2img + (size_t)(nt*144 + sp*72)*9216; }
    else { Abase = (const char*)((sp==2?cr:ci) + (size_t)mt*128*SEQ); Bbase = abimg + (size_t)(((sp-2)*4+nt)*64)*9216; }
    const int W = sp<2 ? K2WM : K2WS, rho = K2RHO;
    if(wid<6) k2_loader<5>(smem, Abase, sp<2, Bbase, T, wid-4, lane, W, rho);
    else      k2_loader<4>(smem, Abase, sp<2, Bbase, T, wid-4, lane, W, rho);
  } else {
    const int mi = wid>>1, ki = wid&1;
    const int fr = lane&15, g = lane>>4;
    int aoff0, aoff1;
    if(sp<2){ aoff0 = 2*mi*4096 + (4*ki+g)*512 + fr*16; aoff1 = aoff0 + 256; }
    else { aoff0 = (64*mi + fr)*128 + (((4*ki+g) ^ (fr>>1))*16); aoff1 = aoff0 + 2048; }
    const int q = fr>>2, p = lane&3, ph = p>>1;
    const int boff0 = K2_AB + ki*9216 + ph*512 + (8*g + 4*ph + q)*16 + (p&1)*8;
    const int boff1 = K2_AB + ki*9216 + ph*512 + (8*g + 4*(1-ph) + q)*16 + (p&1)*8;
    #pragma unroll
    for(int m=0;m<4;m++)
      #pragma unroll
      for(int n=0;n<9;n++) acc[m][n] = (f32x4){0.f,0.f,0.f,0.f};
    constexpr int BD = K2BD;
    bf16x8 A0[4], A1[4], B[9];
    auto ldA = [&](bf16x8 (&A)[4], int st){ const char* p0 = smem + (st&3)*K2_ST;
      A[0] = *(const bf16x8*)(p0 + aoff0); A[1] = *(const bf16x8*)(p0 + aoff1);
      A[2] = *(const bf16x8*)(p0 + aoff0 + 4096); A[3] = *(const bf16x8*)(p0 + aoff1 + 4096); };
    auto ldB = [&](int st, int n)->bf16x8 { char* p0 = smem + (st&3)*K2_ST + n*1024;
      s16x4 lo = __builtin_amdgcn_ds_read_tr16_b64_v4i16((lds_s16x4)(p0 + boff0));
      s16x4 hi = __builtin_amdgcn_ds_read_tr16_b64_v4i16((lds_s16x4)(p0 + boff1));
      return __builtin_shufflevector(lo,hi,0,1,2,3,4,5,6,7); };
    BARRIER();
    STAMP(1,1);
    ldA(A0,0);
    #pragma unroll
    for(int n=0;n<BD;n++) B[n]=ldB(0,n);
    s16x4 Bl[9], Bh[9];
    #pragma unroll
    for(int n=0;n<BD;n++){ Bl[n] = __builtin_shufflevector(B[n],B[n],0,1,2,3); Bh[n] = __builtin_shufflevector(B[n],B[n],4,5,6,7); }
    const unsigned lb = (unsigned)(size_t)(LAS char*)smem;
    unsigned aA0[2] = {lb + aoff0, lb + aoff0 + 2*K2_ST}, aA1[2] = {lb + aoff1, lb + aoff1 + 2*K2_ST};
    unsigned aB0[2] = {lb + boff0, lb + boff0 + 2*K2_ST}, aB1[2] = {lb + boff1, lb + boff1 + 2*K2_ST};
    #pragma unroll
    for(int i=0;i<2;i++) asm volatile("" : "+v"(aA0[i]), "+v"(aA1[i]), "+v"(aB0[i]), "+v"(aB1[i]));
    auto step = [&](bf16x8 (&Ac)[4], bf16x8 (&An)[4], auto STC){
      constexpr int st = decltype(STC)::v, sn = (st+1)&3;
      #pragma unroll
      for(int n=0;n<9;n++){
        if(n+BD==9) BARRIER();
        const int np = n+BD<9 ? n+BD : n+BD-9;
        const int sb = n+BD<9 ? st : sn;
        const int ob = (sb&1)*K2_ST + np*1024;
        const bf16x8 Bc = __builtin_shufflevector(Bl[n],Bh[n],0,1,2,3,4,5,6,7);
        #pragma unroll
        for(int m=0;m<4;m++){
          acc[m][n] = __builtin_amdgcn_mfma_f32_16x16x32_bf16(Bc, Ac[m], acc[m][n], 0,0,0);
          SB();
          if(m==0) Bl[np] = __builtin_amdgcn_ds_read_tr16_b64_v4i16((lds_s16x4)((LAS char*)(aB0[sb>>1]) + ob));
          if(m==1) Bh[np] = __builtin_amdgcn_ds_read_tr16_b64_v4i16((lds_s16x4)((LAS char*)(aB1[sb>>1]) + ob));
          if(m==2 && n+BD>=9 && n+BD<=12){ const int a = n+BD-9;
            An[a] = *(const LAS bf16x8*)((LAS char*)((a&1) ? aA1[sn>>1] : aA0[sn>>1]) + (sn&1)*K2_ST + (a>>1)*4096); }
          SB();
        }
      }
    };
    for(int t=0;t<T;t+=4){ step(A0,A1,IC<0>{}); step(A1,A0,IC<1>{}); step(A0,A1,IC<2>{}); step(A1,A0,IC<3>{}); }
  }
  STAMP(1,2);
  BARRIER();
  if(wid<4){
    const int ki = wid&1;
    char* dst = smem + (wid^1)*18432 + lane*16;
    #pragma unroll
    for(int j=0;j<2;j++)
      #pragma unroll
      for(int n=0;n<9;n++){ f32x4 v = ki ? acc[j][n] : acc[2+j][n]; *(f32x4*)(dst + (j*9+n)*1024) = v; }
  }
  __syncthreads();
  STAMP(1,3);
  f32x4* own = (f32x4*)(smem + 4*18432);
  const rsrc_t rS = mk_rsrc(slab);
  if(wid<4){
    const int ki = wid&1;
    const char* src = smem + wid*18432 + lane*16;
    const unsigned so = (unsigned)(((tile*4 + wid)*4 + sp)*9216) + lane*16;
    #pragma unroll
    for(int n=0;n<9;n++){
      f32x4 v0 = (ki ? acc[2][n] : acc[0][n]) + *(const f32x4*)(src + n*1024);
      f32x4 v1 = (ki ? acc[3][n] : acc[1][n]) + *(const f32x4*)(src + (9+n)*1024);
      if(wid==sp){ own[n*64+lane] = v0; own[(9+n)*64+lane] = v1; }
      else { u32x4 w = {pk_bf16(v0[0],v0[1]), pk_bf16(v0[2],v0[3]), pk_bf16(v1[0],v1[1]), pk_bf16(v1[2],v1[3])};
        #pragma unroll
        for(int e=0;e<4;e++) w[e] = min(w[e], 0xfffffffeu);
        __builtin_amdgcn_raw_buffer_store_b128(w, rS, so + n*1024, 0, 16); }
    }
  }
  STAMP(1,4);
  const int fr = lane&15, g = lane>>4;
  const int nu = wid==4 ? 2 : 1;
  u32x2 xv[2][2], bv[2];
  #pragma unroll
  for(int i=0;i<2;i++){ if(i<nu){ const int n = i ? 8 : wid; const int col = nt*144 + 16*n + 4*g;
      #pragma unroll
      for(int j=0;j<2;j++) xv[i][j] = *(const u32x2*)(x + (size_t)(mt*128 + 32*sp + 16*j + fr)*DM + col);
      bv[i] = *(const u32x2*)(b2 + col); } }
  u32x4 pp[2][3];
  __syncthreads();
  { int spins = 0; unsigned done = 0;
    while(true){
      #pragma unroll
      for(int q=0;q<3;q++){ if(!((done>>q)&1)){ const int ss = q + (q>=sp ? 1 : 0);
        #pragma unroll
        for(int i=0;i<2;i++){ if(i<nu){ const int n = i ? 8 : wid;
          pp[i][q] = __builtin_amdgcn_raw_buffer_load_b128(rS, (unsigned)(((tile*4 + sp)*4 + ss)*9216) + n*1024 + lane*16, 0, 16); } } } }
      #pragma unroll
      for(int q=0;q<3;q++){ if(!((done>>q)&1)){ unsigned all = 0;
        #pragma unroll
        for(int i=0;i<2;i++){ if(i<nu) all = max(all, max(max(pp[i][q][0],pp[i][q][1]), max(pp[i][q][2],pp[i][q][3]))); }
        if(__all(all != 0xffffffffu)) done |= 1u<<q; } }
      if(done==7u) break;
      if(++spins > (1<<20)) break;
      __builtin_amdgcn_s_sleep(PSLEEP);
    } }
  STAMP(1,5);
  #pragma unroll
  for(int i=0;i<2;i++){ if(i<nu){ const int n = i ? 8 : wid; const int col = nt*144 + 16*n + 4*g;
    const float bf[4] = {__uint_as_float(bv[i].x<<16), __uint_as_float(bv[i].x&0xffff0000u), __uint_as_float(bv[i].y<<16), __uint_as_float(bv[i].y&0xffff0000u)};
    #pragma unroll
    for(int j=0;j<2;j++){
      const f32x4 ow = own[(j*9+n)*64+lane];
      f32x4 p[3];
      #pragma unroll
      for(int q=0;q<3;q++){ const unsigned a = pp[i][q][2*j], b = pp[i][q][2*j+1];
        p[q] = (f32x4){__uint_as_float(a<<16), __uint_as_float(a&0xffff0000u), __uint_as_float(b<<16), __uint_as_float(b&0xffff0000u)}; }
      f32x4 mm, sv;
      if(sp<2){ mm = ow + p[0]; sv = p[1] + p[2]; }
      else    { mm = p[0] + p[1]; sv = ow + p[2]; }
      const float xf[4] = {__uint_as_float(xv[i][j].x<<16), __uint_as_float(xv[i][j].x&0xffff0000u), __uint_as_float(xv[i][j].y<<16), __uint_as_float(xv[i][j].y&0xffff0000u)};
      float o[4];
      #pragma unroll
      for(int e=0;e<4;e++){ float m = mm[e]+bf[e];
        float gt = m * __builtin_amdgcn_rcpf(1.f + __builtin_amdgcn_exp2f(-1.44269504f*m));
        o[e] = xf[e] + sv[e]*gt; }
      *(u32x2*)(out + (size_t)(mt*128 + 32*sp + 16*j + fr)*DM + col) = (u32x2){pk_bf16(o[0],o[1]), pk_bf16(o[2],o[3])};
    } } }
  STAMP(1,6);
}

extern "C" void kernel_launch(void* const* d_in, const int* in_sizes, int n_in,
                              void* d_out, int out_size, void* d_ws, size_t ws_size,
                              hipStream_t stream) {
  const u16* x=(const u16*)d_in[0]; const u16* cr=(const u16*)d_in[1]; const u16* ci=(const u16*)d_in[2];
  const u16* wre=(const u16*)d_in[3]; const u16* wim=(const u16*)d_in[4];
  const u16* w1=(const u16*)d_in[6]; const u16* b1=(const u16*)d_in[7]; const u16* w2=(const u16*)d_in[8]; const u16* b2=(const u16*)d_in[9];
  char* ws=(char*)d_ws;
  char* himg = ws; char* abimg = ws + (20<<20); char* w2img = ws + (26<<20);
  float* slab=(float*)(ws+(32<<20)); unsigned* cnt=(unsigned*)(ws+(52<<20));
  unsigned long long* stamps = (unsigned long long*)(ws+(128<<20));
  (void)hipFuncSetAttribute((const void*)k1_kernel, hipFuncAttributeMaxDynamicSharedMemorySize, K1_LDS);
  (void)hipFuncSetAttribute((const void*)k2_kernel, hipFuncAttributeMaxDynamicSharedMemorySize, K2_LDS);
  k1_kernel<<<256,512,K1_LDS,stream>>>(x,wre,wim,w1,b1,w2,himg,abimg,w2img,(unsigned*)slab,stamps,cr,ci);
  k2_kernel<<<256,512,K2_LDS,stream>>>(x,cr,ci,abimg,himg,w2img,b2,slab,cnt,(u16*)d_out,stamps);
}
```

```cpp
#include <hip/hip_runtime.h>
#include <hip/hip_bf16.h>
typedef unsigned short u16;
typedef short bf16x8 __attribute__((ext_vector_type(8)));
typedef short s16x4 __attribute__((ext_vector_type(4)));
typedef float f32x4 __attribute__((ext_vector_type(4)));
typedef float f32x2 __attribute__((ext_vector_type(2)));
typedef float f32x16 __attribute__((ext_vector_type(16)));
typedef unsigned u32x4 __attribute__((ext_vector_type(4)));
typedef unsigned u32x2 __attribute__((ext_vector_type(2)));
typedef __bf16 bf2 __attribute__((ext_vector_type(2)));
#define LAS __attribute__((address_space(3)))
typedef LAS s16x4* lds_s16x4;
typedef LAS void* lds_vp;
typedef __amdgpu_buffer_rsrc_t rsrc_t;
#define DI __device__ __forceinline__
DI float bf2f(u16 v){ return __uint_as_float(((unsigned)v)<<16); }
DI u16 f2bf(float f){ unsigned u=__float_as_uint(f); u += 0x7fffu + ((u>>16)&1u); return (u16)(u>>16); }
DI unsigned pk_bf16(float lo, float hi){ bf2 v; v[0]=(__bf16)lo; v[1]=(__bf16)hi; return __builtin_bit_cast(unsigned,v); }
DI float opq(float x){ asm("" : "+v"(x)); return x; }
DI rsrc_t mk_rsrc(const void* p){ return __builtin_amdgcn_make_buffer_rsrc((void*)p, 0, 0x7fffffff, 0x00020000); }
#define ROT 0
#define PRIO 1
#define K2BD 5
#define CPF 1
#define PSLEEP 2
#define DEEP 0
#define ROTF ((mb + 3*nb + (nb/3)) % 9)
#define K2WM 12
#define K2WS 8
#define K2RHO 0
#define K2MIX 0
#define PFD 3
constexpr int SEQ=2048, DM=576, FF=4608;

#define STAMP(K,i)
#define SB() __builtin_amdgcn_sched_barrier(0)
#define BARRIER() do{ SB(); asm volatile("":::"memory"); __builtin_amdgcn_s_barrier(); asm volatile("":::"memory"); SB(); }while(0)
#define WAITV(n) asm volatile("s_waitcnt vmcnt(%0)"::"n"(n):"memory")
#define WAITL0() asm volatile("s_waitcnt lgkmcnt(0)":::"memory")

DI void dma16(rsrc_t r, unsigned lds_addr, unsigned voff, unsigned soff){
  asm volatile("s_mov_b32 m0, %0\n\ts_nop 4\n\tbuffer_load_dwordx4 %1, %2, %3 offen lds"
               :: "s"(lds_addr), "v"(voff), "s"(r), "s"(soff) : "memory");
}

template<int LOFF, int SOFF>
DI void dma16i(rsrc_t r, unsigned lds_base, unsigned voff){
  unsigned tmp;
  asm volatile("s_add_u32 m0, %1, %2\n\ts_mov_b32 %0, %3\n\ts_nop 3\n\tbuffer_load_dwordx4 %4, %5, %0 offen lds"
               : "=&s"(tmp) : "s"(lds_base), "i"(LOFF), "i"(SOFF), "v"(voff), "s"(r) : "memory", "scc");
}
template<int N> struct IC { static constexpr int v = N; };
template<int B, int E, class Fn> DI void static_for(Fn&& f){ if constexpr (B<E){ f(IC<B>{}); static_for<B+1,E>(f); } }
constexpr int K1_U = 73728;
constexpr int K1_STAT = 2*K1_U;
constexpr int K1_B1   = K1_STAT + 2048;
constexpr int K1_CSP  = K1_B1 + 1536;
constexpr int K1_DUM  = K1_CSP + 2048;
constexpr int K1_TAG  = K1_DUM + 2048;
constexpr int K1_W2S  = K1_TAG + 256;
constexpr int K1_LDS  = K1_W2S + 8192;

template<int LOFF>
DI void dma16f(rsrc_t r, unsigned lds_base, unsigned voff, unsigned soff){
  asm volatile("s_add_u32 m0, %0, %1\n\ts_nop 3\n\tbuffer_load_dwordx4 %2, %3, %4 offen lds"
               :: "s"(lds_base), "i"(LOFF), "v"(voff), "s"(r), "s"(soff) : "memory", "scc");
}
typedef unsigned long long u64;
DI void inbox_fill(const void* slab, int tq, int wid, int lane){
  const u32x4 ff = {0xffffffffu,0xffffffffu,0xffffffffu,0xffffffffu};
  const rsrc_t rq = mk_rsrc(slab);
  #pragma unroll
  for(int i=0;i<4;i++){ const int p = wid + 8*i; if(p<27){ const int k3 = p/9, n = p - k3*9, src = k3 + (k3>=(tq&3) ? 1 : 0);
      __builtin_amdgcn_raw_buffer_store_b128(ff, rq, (unsigned)((tq*4 + src)*9216 + n*1024) + lane*16, 0, 16); } }
}
DI void ctl_fetch(const char* ctl, unsigned lds_dst, int lane){
  const rsrc_t rb = mk_rsrc(ctl);
  asm volatile("s_mov_b32 m0, %0\n\ts_nop 4\n\tbuffer_load_dword %1, %2, 0 offen sc1 lds" :: "s"(lds_dst), "v"(lane*4), "s"(rb) : "memory");
}
DI void flag_set(char* ctl, unsigned off, u64 tag){ __hip_atomic_store((u64*)(ctl + off), tag, __ATOMIC_RELAXED, __HIP_MEMORY_SCOPE_AGENT); }
DI void flag_wait(const char* ctl, unsigned off, bool need, u64 tag){
  const rsrc_t rb = mk_rsrc(ctl);
  for(int spins=0; spins<(1<<20); spins++){
    const u32x2 v = __builtin_amdgcn_raw_buffer_load_b64(rb, off, 0, 16);
    const bool ok = !need || (v[0]==(unsigned)tag && v[1]==(unsigned)(tag>>32));
    if(__all(ok)) break;
    __builtin_amdgcn_s_sleep(4);
  }
}
DI int wg_of(int mb, int nb){ const int L = (mb>>3)*120 + nb*8 + (mb&7); const int xc = L/30; return (L - xc*30)*8 + xc; }
DI void touch_lines(const u16* p, unsigned lds_dummy, int lane){
  const rsrc_t rb = mk_rsrc(p);
  asm volatile("s_mov_b32 m0, %0\n\ts_nop 4\n\tbuffer_load_dword %1, %2, 0 offen lds" :: "s"(lds_dummy), "v"(lane*128), "s"(rb) : "memory");
}
template<bool AB, int fb>
DI void k1_body(char* smem, const u16* x, const u16* wre, const u16* wim, const u16* w1, const u16* b1, const u16* w2,
                char* himg, char* abimg, char* w2img, int mb, int nb, int L, int wid, int lane, unsigned long long* stamps, const u16* cpf, const void* slab, char* ctl, int ibx){
  const unsigned lbase = (unsigned)(size_t)(LAS char*)smem;
  const int w4 = wid&3;
  const int r = lane&31, hh = lane>>5;
  constexpr int WROW = AB ? 1152 : FF*2;
  constexpr int WKT  = AB ? 128 : 64*FF*2;
  const rsrc_t rX = mk_rsrc(x + (size_t)mb*128*DM);
  const unsigned voR0 = (lane>>3)*1152 + (((lane&7) ^ (lane>>4))*16);
  const unsigned voW0 = AB ? voR0 : (unsigned)((lane>>3)*(FF*2) + (((lane&7) ^ (((lane>>4)&1)<<2))*16));
  auto wrsrc = [&](int u)->rsrc_t {
    const int uc = u<=5 ? u : 5; const u16* p;
    if constexpr (AB){ int ec = (nb-12)*384 + uc*64; p = ec>=576 ? wim + (size_t)(ec-576)*DM : wre + (size_t)ec*DM; }
    else p = w1 + nb*384 + uc*64;
    return __builtin_amdgcn_make_buffer_rsrc((void*)p, 0, u<=5 ? 0x7fffffff : 0, 0x00020000);
  };
  if constexpr (!AB && fb==0){ if(wid<3){
    const rsrc_t rb = mk_rsrc(b1 + nb*384 + wid*128);
    asm volatile("s_mov_b32 m0, %0\n\ts_nop 4\n\tbuffer_load_dword %1, %2, 0 offen lds" :: "s"(lbase + K1_B1 + wid*256), "v"(lane*4), "s"(rb) : "memory"); } }
  if constexpr (fb==0){ if(wid==3) ctl_fetch(ctl, lbase + K1_TAG, lane); }
  const unsigned voXa = voR0 + wid*16*1152, voXb = (voR0^64) + (wid*16+8)*1152;
  const unsigned voS = (AB ? (voW0 ^ ((wid&1)<<6)) : voW0) + wid*8*WROW;
  const unsigned lbX = lbase + wid*2048, lbS = lbase + wid*1024;
  unsigned kto[9];
  { const int rho = ROTF;
    #pragma unroll
    for(int i=0;i<9;i++){ int t = i + rho; if(t>=9) t -= 9; kto[i] = t; } }
  #define DMAX(kt) do{ dma16f<((3*(kt))%18)*8192>(rX, lbX, voXa, kto[kt]*128); dma16f<((3*(kt))%18)*8192+1024>(rX, lbX, voXb, kto[kt]*128); }while(0)
  #define DMAW(rw,slot,kt) dma16f<(slot)*8192>(rw, lbS, voS, kto[kt]*WKT)
  const rsrc_t rw0 = wrsrc(0), rw1 = wrsrc(1), rw2 = wrsrc(2);
  DMAX(0); DMAW(rw0,2,0);
  DMAX(1); DMAW(rw0,5,1); DMAX(2); DMAW(rw0,8,2);

  unsigned xoff[4];
  unsigned woff[4];
  #pragma unroll
  for(int sp=0;sp<4;sp++){
    int s = sp ^ w4;
    int row = 32*w4 + r;
    xoff[sp] = lbase + row*128 + (((2*s+hh) ^ ((row>>1)&7))<<4);
    if constexpr (AB){ int wr = 32*fb + r; woff[sp] = wr*128 + (((2*s+hh) ^ ((wr>>1)&7))<<4); }
    else { int q=(lane&15)>>2, p=lane&3, blk=(lane>>4)&1; int wr = 16*s + 8*hh + q;
           woff[sp] = wr*128 + ((64*fb + 32*blk + 8*p) ^ (((q>>1)&1)<<6)); }
    woff[sp] += lbase;
  }
  auto wadv = [&](int d){
    #pragma unroll
    for(int sp=0;sp<4;sp++){ woff[sp] += d; asm volatile("" : "+v"(woff[sp])); } };
  auto ldW = [&](int off, int sp)->bf16x8 {
    LAS char* p = (LAS char*)(woff[sp]) + off;
    if constexpr (AB){ return *(const LAS bf16x8*)p; }
    else { s16x4 lo = __builtin_amdgcn_ds_read_tr16_b64_v4i16((lds_s16x4)p);
           s16x4 hi = __builtin_amdgcn_ds_read_tr16_b64_v4i16((lds_s16x4)(p + 512));
           return __builtin_shufflevector(lo,hi,0,1,2,3,4,5,6,7); }
  };
  const short selv = ((lane&15) == ((lane>>4)&1)) ? (short)0x3f80 : (short)0;
  const bf16x8 sel = {selv,selv,selv,selv,selv,selv,selv,selv};

  bf16x8 X[36];
  f32x16 P, Q;
  f32x4 cs = {0.f,0.f,0.f,0.f}, sx = {0.f,0.f,0.f,0.f};
  #pragma unroll
  for(int i=0;i<16;i++){ P[i]=0.f; Q[i]=0.f; }
  constexpr int PD = PFD;
  bf16x8 pw[PD+1];
  STAMP(0,1);
  #define TW(i)
  TW(0);
  if constexpr (fb==1) __builtin_amdgcn_s_setprio(1);
  bf16x8 wf[2][4];
  auto smma = [&](auto KT, auto RDC){
    constexpr int kt = decltype(KT)::v; constexpr bool RD = decltype(RDC)::v;
    #pragma unroll
    for(int sp=0;sp<4;sp++){
      P = __builtin_amdgcn_mfma_f32_32x32x16_bf16(wf[kt&1][sp], X[kt*4+sp], P, 0,0,0);
      if constexpr (RD){ SB(); X[(kt+1)*4+sp] = *(const LAS bf16x8*)((LAS char*)(xoff[sp]) + (((kt+1)&1) ? 24576 : 0)); wf[(kt+1)&1][sp] = ldW(16384,sp); SB(); }
      if(sp==0) cs = __builtin_amdgcn_mfma_f32_16x16x32_bf16(sel, wf[kt&1][0], cs, 0,0,0);
      if((sp>>1)==fb){ Q = __builtin_amdgcn_mfma_f32_32x32x16_bf16(X[kt*4+sp], X[kt*4+sp], Q, 0,0,0);
                       sx = __builtin_amdgcn_mfma_f32_16x16x32_bf16(sel, X[kt*4+sp], sx, 0,0,0); }
    }
    asm volatile("" : "+v"(Q));
    asm volatile("" : "+v"(P), "+v"(sx), "+v"(cs));
  };
  auto sstep = [&](auto KT){
    constexpr int kt = decltype(KT)::v;
    WAITV(6);
    WAITL0();
    BARRIER();
    TW(1+kt);
    auto lds = [&](){
      #pragma unroll
      for(int sp=0;sp<4;sp++){ X[kt*4+sp] = *(const LAS bf16x8*)((LAS char*)(xoff[sp]) + ((kt&1) ? 24576 : 0)); wf[kt&1][sp] = ldW(16384,sp); }
    };
    auto dmas = [&](){
      if constexpr (kt<=5){ DMAX(kt+3); DMAW(rw0,(3*(kt+3)+2)%18,kt+3); }
      else { DMAW(rw1,9+3*(kt-6),3*(kt-6)); DMAW(rw1,10+3*(kt-6),3*(kt-6)+1); DMAW(rw1,11+3*(kt-6),3*(kt-6)+2); }
    };
    if constexpr (kt==0){ lds(); SB(); dmas(); }
    else if constexpr (fb==0){ dmas(); SB(); lds(); SB(); smma(IC<kt-1>{}, IC<0>{}); }
    else     { smma(IC<kt-1>{}, IC<1>{}); SB(); dmas(); }
    wadv(kt==5 ? -5*24576 : 24576);
    if(kt&1){
      #pragma unroll
      for(int sp=0;sp<4;sp++){ xoff[sp] += (kt==5 ? -4*24576 : 2*24576); asm volatile("" : "+v"(xoff[sp])); } }
  };
  sstep(IC<0>{}); sstep(IC<1>{}); sstep(IC<2>{}); sstep(IC<3>{}); sstep(IC<4>{}); sstep(IC<5>{}); sstep(IC<6>{}); sstep(IC<7>{}); sstep(IC<8>{});
  smma(IC<8>{}, IC<0>{});
  if constexpr (fb==1) __builtin_amdgcn_s_setprio(0);
  auto put_cs = [&](int par){
    if(lane<16){ float* d = (float*)(smem + K1_CSP + par*1024 + wid*128 + lane*4); d[0] = cs[0]; d[16] = cs[1]; }
  };
  put_cs(0);
  if(lane<16){ float* d = (float*)(smem + K1_STAT + fb*512 + (32*w4+lane)*4); d[0] = sx[0]; d[16] = sx[1]; }
  { const int dr = (r&3) + 4*(r>>3); float dg = Q[0];
    #pragma unroll
    for(int i=1;i<16;i++) dg = (dr==i) ? Q[i] : dg;
    if(hh == ((r>>2)&1)) *(float*)(smem + K1_STAT + 1024 + fb*512 + (32*w4+r)*4) = dg; }
  WAITV(0); WAITL0();
  BARRIER();
  STAMP(0,2);
  #pragma unroll
  for(int i=0;i<PD;i++) pw[i] = ldW(0,i);
  float rr, irr, K0, K1c; bf16x8 Bext;
  { const float* sp_ = (const float*)(smem + K1_STAT + (32*w4+r)*4); const float s1 = sp_[0] + sp_[128], s2 = sp_[256] + sp_[384];
    float mu = s1*(1.f/DM); float var = fmaxf(s2*(1.f/DM) - mu*mu, 0.f);
    float sd = __builtin_amdgcn_sqrtf(var + 1e-5f);
    irr = sd;
    if constexpr (!AB){ float v2 = var*__builtin_amdgcn_rcpf(var + 1e-5f); irr = sd*__builtin_amdgcn_sqrtf(v2 + 1e-5f); }
    rr = __builtin_amdgcn_rcpf(irr);
    K0 = -2.30220820f*rr; K1c = -0.10294324f*rr*rr*rr;
    unsigned i0 = pk_bf16(irr,irr) & 0xffff0000u; unsigned m0 = pk_bf16(-mu,-mu) & 0xffff0000u;
    unsigned wi = pk_bf16(irr, irr - __uint_as_float(i0)), wm = pk_bf16(-mu, -mu - __uint_as_float(m0));
    u32x4 bx = {wi, wm, wm, 0u};
    if(hh) bx = (u32x4){0u,0u,0u,0u};
    Bext = __builtin_bit_cast(bf16x8, bx); }
  const rsrc_t rH = mk_rsrc(AB ? abimg : himg);

  float cp4[4]; unsigned bw = 0; float to[2]; u32x2 pk[2];
  unsigned obase; float a_r = rr; int ce0=0, ri=0;
  auto epi_load = [&](int u){
    if constexpr (AB){ int ec = (nb-12)*384 + u*64; ri = ec>=576; ce0 = ((ec - ri*576)>>3) + 4*fb; a_r = ri ? -rr : rr; obase = hh*512 + ((r ^ (4*hh))*16); }
    else { obase = (unsigned)(((mb*72 + nb*6 + u)*4 + w4)*4096 + fb*2048) + lane*16;
           bw = *(const u16*)(smem + K1_B1 + u*128 + fb*64 + r*2); }
    const float* cp = (const float*)(smem + K1_CSP + (u&1)*1024 + fb*512);
    cp4[0] = cp[r]; cp4[1] = cp[32+r]; cp4[2] = cp[64+r]; cp4[3] = cp[96+r];
  };
  auto epi_fix = [&](f32x16& O){
    float c = opq(opq(cp4[0]+cp4[1]) + opq(cp4[2]+cp4[3]));
    unsigned c0 = pk_bf16(c,c); float c1 = c - __uint_as_float(c0 & 0xffff0000u);
    u32x4 ax = {bw*0x10001u, c0, pk_bf16(c1,c1), 0u};
    O = __builtin_amdgcn_mfma_f32_32x32x16_bf16(__builtin_bit_cast(bf16x8,ax), Bext, O, 0,0,0);
  };
  float tq[3]; u32x4 vst; unsigned ost;
  auto epi_q = [&](const f32x16& O, int e, int stg){
    float& t = tq[e%3];
    if constexpr (AB){
      if(stg==3) to[e&1] = opq(a_r*O[e]);
    } else {
      const float v = O[e];
      if(stg==0) t = opq(fmaf(opq(v*v), K1c, K0));
      if(stg==1) t = opq(__builtin_amdgcn_exp2f(opq(v*t)));
      if(stg==2) t = opq(__builtin_amdgcn_rcpf(opq(fmaf(t, irr, irr))));
      if(stg==3) to[e&1] = opq(v*t);
    }
    if(stg==3){
      if(e&1) pk[(e>>2)&1][(e>>1)&1] = pk_bf16(to[0],to[1]);
      if((e&7)==7){
        auto s0 = __builtin_amdgcn_permlane32_swap(pk[0][0], pk[1][0], false, false);
        auto s1 = __builtin_amdgcn_permlane32_swap(pk[0][1], pk[1][1], false, false);
        u32x4 v = {s0[0], s1[0], s0[1], s1[1]};
        unsigned so;
        if constexpr (AB){ int ce = ce0 + (e>>3)*2; int nt = ce/18, cin = ce - nt*18; so = (unsigned)((((ri*4+nt)*64 + mb*4 + w4)*18 + cin)*512); }
        else so = (e>>3)*1024;
        vst = v; ost = obase + so;
      }
    }
  };
  auto epi_store = [&](){ __builtin_amdgcn_raw_buffer_store_b128(vst, rH, ost, 0, 16); };
  auto epi_step = [&](const f32x16& O, int kk){
    if(kk<4) return;
    const int e1 = (kk-2)>>1, e0 = e1-1;
    if(kk<8) epi_q(O,0,kk-4);
    if(e0>=1) epi_q(O,e0,kk-2*e0-2);
    if(e1<=15) epi_q(O,e1,kk-2*e1-2);
  };
  #define TS(i)
  rsrc_t rwa = rw2, rwb = wrsrc(3);
  auto interval = [&](f32x16& N, f32x16& O, int u, auto ODDC){
    constexpr int ODD = decltype(ODDC)::v & 1; constexpr bool FIRST = decltype(ODDC)::v == 3;
    TS(0);
    epi_load(u-1);
    const int rli = (L>=120 ? L-120 : L) < 108 ? (L>=120 ? L-120 : L) : 107, rcb = rli/12, rkb = (L>=120 ? 72 : 0) + (rli - rcb*12)*6 + fb;
    if(!FIRST && u<=4){
      const u32x4 v = *(const LAS u32x4*)((LAS char*)(lbase + K1_W2S + fb*4096 + r*128 + (((2*w4+hh) ^ ((r>>1)&7))<<4)));
      const int ce = rcb*8 + 2*w4, nt_ = ce/18, cin = ce - nt_*18;
      __builtin_amdgcn_raw_buffer_store_b128(v, mk_rsrc(w2img), hh*512 + ((r ^ (4*hh))*16) + ((nt_*144 + rkb + 2*(u-2))*18 + cin)*512, 0, 16);
    }
    if(fb==0){ if(u==w4+1) touch_lines(cpf, lbase + K1_DUM + wid*256, lane); }
    static_for<0,36>([&](auto KKC){
      constexpr int kk = decltype(KKC)::v;
      constexpr int kp = kk+PD;
      if(kp==20) wadv(5*8192);
      if(kp==36) wadv(ODD ? -14*8192 : 4*8192);
      pw[kp%(PD+1)] = ldW(kp<20 ? (kp>>2)*8192 : (kp<36 ? ((kp-20)>>2)*8192 : 0), kp&3);
      if constexpr (FIRST){
        if(kk==0+fb) dma16f<0*8192>(rwa,lbS,voS,kto[0]*WKT);
        if(kk==2+fb) dma16f<1*8192>(rwa,lbS,voS,kto[1]*WKT);
        if(kk==4+fb) dma16f<2*8192>(rwa,lbS,voS,kto[2]*WKT);
        if(kk==6+fb) dma16f<3*8192>(rwa,lbS,voS,kto[3]*WKT);
        if(kk==8+fb) dma16f<4*8192>(rwa,lbS,voS,kto[4]*WKT);
        if(kk==10+fb) dma16f<5*8192>(rwa,lbS,voS,kto[5]*WKT);
        if(kk==12+fb) dma16f<6*8192>(rwa,lbS,voS,kto[6]*WKT);
        if(kk==14+fb) dma16f<7*8192>(rwa,lbS,voS,kto[7]*WKT);
        if(kk==16+fb) dma16f<8*8192>(rwa,lbS,voS,kto[8]*WKT);
      } else {
      if(kk==1+2*fb) dma16f<(1-ODD)*K1_U+5*8192>(rwa,lbS,voS,kto[5]*WKT);
      if(kk==5+2*fb) dma16f<(1-ODD)*K1_U+6*8192>(rwa,lbS,voS,kto[6]*WKT);
      if(kk==9+2*fb) dma16f<(1-ODD)*K1_U+7*8192>(rwa,lbS,voS,kto[7]*WKT);
      if(kk==13+2*fb) dma16f<(1-ODD)*K1_U+8*8192>(rwa,lbS,voS,kto[8]*WKT);
      }
      if(kk==21+fb ) dma16f<ODD*K1_U+0*8192>(rwb,lbS,voS,kto[0]*WKT);
      if(kk==24+fb ) dma16f<ODD*K1_U+1*8192>(rwb,lbS,voS,kto[1]*WKT);
      if(kk==27+fb ) dma16f<ODD*K1_U+2*8192>(rwb,lbS,voS,kto[2]*WKT);
      if(kk==30+fb ) dma16f<ODD*K1_U+3*8192>(rwb,lbS,voS,kto[3]*WKT);
      if(kk==32+fb ) dma16f<ODD*K1_U+4*8192>(rwb,lbS,voS,kto[4]*WKT);
      SB();
      if(kk==0){ f32x16 z;
        #pragma unroll
        for(int i=0;i<16;i++) z[i]=0.f;
        N = __builtin_amdgcn_mfma_f32_32x32x16_bf16(pw[0], X[0], z, 0,0,0);
        cs = __builtin_amdgcn_mfma_f32_16x16x32_bf16(sel, pw[0], (f32x4){0.f,0.f,0.f,0.f}, 0,0,0); }
      else { N = __builtin_amdgcn_mfma_f32_32x32x16_bf16(pw[kk%(PD+1)], X[kk], N, 0,0,0);
        if((kk&3)==0) cs = __builtin_amdgcn_mfma_f32_16x16x32_bf16(sel, pw[kk%(PD+1)], cs, 0,0,0);
      }
      if(kk==2) epi_fix(O);
      epi_step(O, kk);
      if(kk==20) epi_store();
      if(kk==0 && !FIRST) epi_store();
      if(kk==34){ put_cs(ODD); rwa = rwb; rwb = wrsrc(u+3); }
      SB();
      if(kk==19){ TS(1); if constexpr (FIRST) WAITV(8); else WAITV(4); BARRIER(); TS(2);
        if(u<=3) dma16(mk_rsrc(w2), lbase + K1_W2S + wid*1024, ((lane>>3)*1152 + (((lane&7) ^ (lane>>4) ^ ((w4&1)<<2))*16)), (unsigned)(((rkb + 2*(u-1))*32 + w4*8)*1152 + rcb*128));
        if(fb==1){ if(u==(w4<3 ? w4+1 : 3)) touch_lines(cpf, lbase + K1_DUM + wid*256, lane); } }
      if(kk==34 && !ODD){ if(u==4) WAITV(0); }
      if(kk==35){ TS(3); WAITV(5); asm volatile("s_waitcnt lgkmcnt(%0)"::"n"(AB ? 1 : 2):"memory"); BARRIER();
        if constexpr (!ODD && fb==0){ if(u==4 && wid==0){ const u64 tag = *(const u64*)(smem + K1_TAG) + 1; if(lane==0) flag_set(ctl, 4096 + blockIdx.x*8, tag); } } }
    });
    TS(10);
    STAMP(0,2+u);
  };
  interval(Q, P, 1, IC<3>{});
  #pragma unroll 1
  for(int it=1; it<3; it++){
    interval(P, Q, 2*it, IC<0>{});
    interval(Q, P, 2*it+1, IC<1>{});
  }
  epi_store();
  inbox_fill(slab, ibx, wid, lane);
  epi_load(5);
  epi_fix(Q);
  static_for<4,36>([&](auto KKC){ epi_step(Q,decltype(KKC)::v); if(decltype(KKC)::v==19 || decltype(KKC)::v==35) epi_store(); });
  STAMP(0,10);
}

constexpr int K2_AB = 128*128;
constexpr int K2_BB = 64*288;
constexpr int K2_ST = K2_AB + K2_BB;
constexpr int K2_LDS = K2_ST*4;

DI void dma16c(rsrc_t r, unsigned lds_addr, unsigned voff, unsigned soff){
  asm volatile("s_mov_b32 m0, %0\n\ts_nop 4\n\tbuffer_load_dwordx4 %1, %2, %3 offen sc1 lds"
               :: "s"(lds_addr), "v"(voff), "s"(r), "s"(soff) : "memory");
}
template<int NP_B>
DI void k2_loader(char* smem, const char* Abase, bool lin, const char* Bbase, int T, int lw, int lane,
                  const char* ctl, u64 tag, unsigned w0off, bool w0need, unsigned w1off, bool w1need){
  const unsigned lbase = (unsigned)(size_t)(LAS char*)smem;
  const rsrc_t rA = mk_rsrc(Abase), rB = mk_rsrc(Bbase);
  unsigned voA[4];
  #pragma unroll
  for(int p=0;p<4;p++){ int row = (lw*4+p)*8 + (lane>>3); int c = (lane&7) ^ ((row>>1)&7);
    voA[p] = lin ? (unsigned)((lw*4+p)*1024 + lane*16) : (unsigned)(row*(SEQ*2) + c*16); }
  const unsigned astep = lin ? 16384u : 128u;
  const int bp0 = lw<2 ? lw*5 : 10 + (lw-2)*4;
  const unsigned voB = bp0*1024 + lane*16;
  constexpr int P = 4+NP_B;
  auto issue = [&](int st){
    int ks = st;
    if(lin){ const int g = st>=18 ? 1 : 0, q = st - 18*g, p = q/3; ks = p*6 + 3*g + (q - 3*p); }
    unsigned dst = lbase + (st&3)*K2_ST;
    #pragma unroll
    for(int p=0;p<4;p++) dma16c(rA, dst + (lw*4+p)*1024, voA[p], ks*astep);
    #pragma unroll
    for(int p=0;p<NP_B;p++) dma16c(rB, dst + K2_AB + (bp0+p)*1024, voB, ks*K2_BB + p*1024);
  };
  flag_wait(ctl, w0off, w0need, tag);
  issue(0); issue(1); issue(2);
  WAITV(2*P);
  BARRIER();
  for(int t=0;t<T;t++){
    if(t+2<T) WAITV(P); else WAITV(0);
    BARRIER();
    if(lin && t==15) flag_wait(ctl, w1off, w1need, tag);
    if(t+3<T) issue(t+3);
  }
}

__global__ __launch_bounds__(512,2) void fused_kernel(const u16* __restrict__ x, const u16* __restrict__ cr, const u16* __restrict__ ci,
    const u16* __restrict__ wre, const u16* __restrict__ wim, const u16* __restrict__ w1, const u16* __restrict__ b1,
    const u16* __restrict__ w2, const u16* __restrict__ b2,
    char* himg, char* abimg, char* w2img, float* slab, char* ctl, u16* out, unsigned long long* stamps){
  extern __shared__ __attribute__((aligned(16))) char smem[];
  const int bid = blockIdx.x; const int xcd = bid&7, idx = bid>>3;
  const int wid = __builtin_amdgcn_readfirstlane(threadIdx.x>>6), lane = threadIdx.x&63;
  const int sp = (0x11330022 >> (4*xcd)) & 3;
  const int mt = (xcd&1)*8 + (idx>>2), nt = idx&3;
  const int tile = mt*4+nt;
  STAMP(0,0);
  const u16* cpf = (bid<128 ? cr : ci) + (size_t)((bid&127)*512 + wid*64)*64;
  if(idx>=30){
    if(wid==3) ctl_fetch(ctl, (unsigned)(size_t)(LAS char*)smem + K1_TAG, lane);
    touch_lines(cpf, (unsigned)(size_t)(LAS char*)smem + wid*256, lane);
    inbox_fill(slab, tile*4+sp, wid, lane);
    STAMP(0,11);
  } else {
  const int L = xcd*30+idx; const int mh = L/120, rem = L - mh*120; const int nb = rem>>3, mb = mh*8 + (rem&7);
  if(nb<12){ if(wid<4) k1_body<false,0>(smem,x,wre,wim,w1,b1,w2,himg,abimg,w2img,mb,nb,L,wid,lane,stamps,cpf,slab,ctl,tile*4+sp);
             else      k1_body<false,1>(smem,x,wre,wim,w1,b1,w2,himg,abimg,w2img,mb,nb,L,wid,lane,stamps,cpf,slab,ctl,tile*4+sp); }
  else     { if(wid<4) k1_body<true ,0>(smem,x,wre,wim,w1,b1,w2,himg,abimg,w2img,mb,nb,L,wid,lane,stamps,cpf,slab,ctl,tile*4+sp);
             else      k1_body<true ,1>(smem,x,wre,wim,w1,b1,w2,himg,abimg,w2img,mb,nb,L,wid,lane,stamps,cpf,slab,ctl,tile*4+sp); }
  }
  WAITV(0); WAITL0();
  BARRIER();
  const u64 tag = *(const u64*)(smem + K1_TAG) + 1;
  if(threadIdx.x==0) flag_set(ctl, 8192 + bid*8, tag);
  const int T = sp<2 ? 36 : 32;
  f32x4 acc[4][9];
  STAMP(1,0);
  if(wid>=4){
    const char* Abase; const char* Bbase;
    if(sp<2){ Abase = himg + (size_t)(mt*72 + sp*36)*16384; Bbase = w2img + (size_t)(nt*144 + sp*72)*9216; }
    else { Abase = (const char*)((sp==2?cr:ci) + (size_t)mt*128*SEQ); Bbase = abimg + (size_t)(((sp-2)*4+nt)*64)*9216; }
    unsigned w0off, w1off = 8192; bool w0need, w1need = false;
    if(sp<2){
      const int pw_ = wg_of(mt, 6*sp + (lane<6 ? lane : 0));
      const int Lw = 120*sp + 24*nt + (lane>=16 && lane<52 ? lane-16 : 0);
      w0need = lane<6 || (lane>=16 && lane<52);
      w0off = 4096 + (lane<16 ? pw_ : (Lw%30)*8 + Lw/30)*8;
      w1need = lane<6; w1off = 8192 + pw_*8;
    } else {
      const int ec = (sp-2)*576 + nt*144 + ((lane&16) ? 143 : 0);
      w0need = lane<32; w0off = 8192 + wg_of(lane&15, 12 + ec/384)*8;
    }
    if(wid<6) k2_loader<5>(smem, Abase, sp<2, Bbase, T, wid-4, lane, ctl, tag, w0off, w0need, w1off, w1need);
    else      k2_loader<4>(smem, Abase, sp<2, Bbase, T, wid-4, lane, ctl, tag, w0off, w0need, w1off, w1need);
  } else {
    const int mi = wid>>1, ki = wid&1;
    const int fr = lane&15, g = lane>>4;
    int aoff0, aoff1;
    if(sp<2){ aoff0 = 2*mi*4096 + (4*ki+g)*512 + fr*16; aoff1 = aoff0 + 256; }
    else { aoff0 = (64*mi + fr)*128 + (((4*ki+g) ^ (fr>>1))*16); aoff1 = aoff0 + 2048; }
    const int q = fr>>2, p = lane&3, ph = p>>1;
    const int boff0 = K2_AB + ki*9216 + ph*512 + (8*g + 4*ph + q)*16 + (p&1)*8;
    const int boff1 = K2_AB + ki*9216 + ph*512 + (8*g + 4*(1-ph) + q)*16 + (p&1)*8;
    #pragma unroll
    for(int m=0;m<4;m++)
      #pragma unroll
      for(int n=0;n<9;n++) acc[m][n] = (f32x4){0.f,0.f,0.f,0.f};
    constexpr int BD = K2BD;
    bf16x8 A0[4], A1[4], B[9];
    auto ldA = [&](bf16x8 (&A)[4], int st){ const char* p0 = smem + (st&3)*K2_ST;
      A[0] = *(const bf16x8*)(p0 + aoff0); A[1] = *(const bf16x8*)(p0 + aoff1);
      A[2] = *(const bf16x8*)(p0 + aoff0 + 4096); A[3] = *(const bf16x8*)(p0 + aoff1 + 4096); };
    auto ldB = [&](int st, int n)->bf16x8 { char* p0 = smem + (st&3)*K2_ST + n*1024;
      s16x4 lo = __builtin_amdgcn_ds_read_tr16_b64_v4i16((lds_s16x4)(p0 + boff0));
      s16x4 hi = __builtin_amdgcn_ds_read_tr16_b64_v4i16((lds_s16x4)(p0 + boff1));
      return __builtin_shufflevector(lo,hi,0,1,2,3,4,5,6,7); };
    BARRIER();
    STAMP(1,1);
    ldA(A0,0);
    #pragma unroll
    for(int n=0;n<BD;n++) B[n]=ldB(0,n);
    s16x4 Bl[9], Bh[9];
    #pragma unroll
    for(int n=0;n<BD;n++){ Bl[n] = __builtin_shufflevector(B[n],B[n],0,1,2,3); Bh[n] = __builtin_shufflevector(B[n],B[n],4,5,6,7); }
    const unsigned lb = (unsigned)(size_t)(LAS char*)smem;
    unsigned aA0[2] = {lb + aoff0, lb + aoff0 + 2*K2_ST}, aA1[2] = {lb + aoff1, lb + aoff1 + 2*K2_ST};
    unsigned aB0[2] = {lb + boff0, lb + boff0 + 2*K2_ST}, aB1[2] = {lb + boff1, lb + boff1 + 2*K2_ST};
    #pragma unroll
    for(int i=0;i<2;i++) asm volatile("" : "+v"(aA0[i]), "+v"(aA1[i]), "+v"(aB0[i]), "+v"(aB1[i]));
    auto step = [&](bf16x8 (&Ac)[4], bf16x8 (&An)[4], auto STC){
      constexpr int st = decltype(STC)::v, sn = (st+1)&3;
      #pragma unroll
      for(int n=0;n<9;n++){
        if(n+BD==9) BARRIER();
        const int np = n+BD<9 ? n+BD : n+BD-9;
        const int sb = n+BD<9 ? st : sn;
        const int ob = (sb&1)*K2_ST + np*1024;
        const bf16x8 Bc = __builtin_shufflevector(Bl[n],Bh[n],0,1,2,3,4,5,6,7);
        #pragma unroll
        for(int m=0;m<4;m++){
          acc[m][n] = __builtin_amdgcn_mfma_f32_16x16x32_bf16(Bc, Ac[m], acc[m][n], 0,0,0);
          SB();
          if(m==0) Bl[np] = __builtin_amdgcn_ds_read_tr16_b64_v4i16((lds_s16x4)((LAS char*)(aB0[sb>>1]) + ob));
          if(m==1) Bh[np] = __builtin_amdgcn_ds_read_tr16_b64_v4i16((lds_s16x4)((LAS char*)(aB1[sb>>1]) + ob));
          if(m==2 && n+BD>=9 && n+BD<=12){ const int a = n+BD-9;
            An[a] = *(const LAS bf16x8*)((LAS char*)((a&1) ? aA1[sn>>1] : aA0[sn>>1]) + (sn&1)*K2_ST + (a>>1)*4096); }
          SB();
        }
      }
    };
    for(int t=0;t<T;t+=4){ step(A0,A1,IC<0>{}); step(A1,A0,IC<1>{}); step(A0,A1,IC<2>{}); step(A1,A0,IC<3>{}); }
  }
  STAMP(1,2);
  BARRIER();
  if(wid<4){
    const int ki = wid&1;
    char* dst = smem + (wid^1)*18432 + lane*16;
    #pragma unroll
    for(int j=0;j<2;j++)
      #pragma unroll
      for(int n=0;n<9;n++){ f32x4 v = ki ? acc[j][n] : acc[2+j][n]; *(f32x4*)(dst + (j*9+n)*1024) = v; }
  }
  __syncthreads();
  STAMP(1,3);
  f32x4* own = (f32x4*)(smem + 4*18432);
  const rsrc_t rS = mk_rsrc(slab);
  if(wid<4){
    const int ki = wid&1;
    const char* src = smem + wid*18432 + lane*16;
    const unsigned so = (unsigned)(((tile*4 + wid)*4 + sp)*9216) + lane*16;
    if(wid!=sp) flag_wait(ctl, 8192 + (idx*8 + ((0x4062 >> (4*wid)) & 7) + (xcd&1))*8, true, tag);
    #pragma unroll
    for(int n=0;n<9;n++){
      f32x4 v0 = (ki ? acc[2][n] : acc[0][n]) + *(const f32x4*)(src + n*1024);
      f32x4 v1 = (ki ? acc[3][n] : acc[1][n]) + *(const f32x4*)(src + (9+n)*1024);
      if(wid==sp){ own[n*64+lane] = v0; own[(9+n)*64+lane] = v1; }
      else { u32x4 w = {pk_bf16(v0[0],v0[1]), pk_bf16(v0[2],v0[3]), pk_bf16(v1[0],v1[1]), pk_bf16(v1[2],v1[3])};
        #pragma unroll
        for(int e=0;e<4;e++) w[e] = min(w[e], 0xfffffffeu);
        __builtin_amdgcn_raw_buffer_store_b128(w, rS, so + n*1024, 0, 16); }
    }
  }
  STAMP(1,4);
  const int fr = lane&15, g = lane>>4;
  const int nu = wid==4 ? 2 : 1;
  u32x2 xv[2][2], bv[2];
  #pragma unroll
  for(int i=0;i<2;i++){ if(i<nu){ const int n = i ? 8 : wid; const int col = nt*144 + 16*n + 4*g;
      #pragma unroll
      for(int j=0;j<2;j++) xv[i][j] = *(const u32x2*)(x + (size_t)(mt*128 + 32*sp + 16*j + fr)*DM + col);
      bv[i] = *(const u32x2*)(b2 + col); } }
  u32x4 pp[2][3];
  __syncthreads();
  { int spins = 0; unsigned done = 0;
    while(true){
      #pragma unroll
      for(int q=0;q<3;q++){ if(!((done>>q)&1)){ const int ss = q + (q>=sp ? 1 : 0);
        #pragma unroll
        for(int i=0;i<2;i++){ if(i<nu){ const int n = i ? 8 : wid;
          pp[i][q] = __builtin_amdgcn_raw_buffer_load_b128(rS, (unsigned)(((tile*4 + sp)*4 + ss)*9216) + n*1024 + lane*16, 0, 16); } } } }
      #pragma unroll
      for(int q=0;q<3;q++){ if(!((done>>q)&1)){ unsigned all = 0;
        #pragma unroll
        for(int i=0;i<2;i++){ if(i<nu) all = max(all, max(max(pp[i][q][0],pp[i][q][1]), max(pp[i][q][2],pp[i][q][3]))); }
        if(__all(all != 0xffffffffu)) done |= 1u<<q; } }
      if(done==7u) break;
      if(++spins > (1<<20)) break;
      __builtin_amdgcn_s_sleep(PSLEEP);
    } }
  STAMP(1,5);
  #pragma unroll
  for(int i=0;i<2;i++){ if(i<nu){ const int n = i ? 8 : wid; const int col = nt*144 + 16*n + 4*g;
    const float bf[4] = {__uint_as_float(bv[i].x<<16), __uint_as_float(bv[i].x&0xffff0000u), __uint_as_float(bv[i].y<<16), __uint_as_float(bv[i].y&0xffff0000u)};
    #pragma unroll
    for(int j=0;j<2;j++){
      const f32x4 ow = own[(j*9+n)*64+lane];
      f32x4 p[3];
      #pragma unroll
      for(int q=0;q<3;q++){ const unsigned a = pp[i][q][2*j], b = pp[i][q][2*j+1];
        p[q] = (f32x4){__uint_as_float(a<<16), __uint_as_float(a&0xffff0000u), __uint_as_float(b<<16), __uint_as_float(b&0xffff0000u)}; }
      f32x4 mm, sv;
      if(sp<2){ mm = ow + p[0]; sv = p[1] + p[2]; }
      else    { mm = p[0] + p[1]; sv = ow + p[2]; }
      const float xf[4] = {__uint_as_float(xv[i][j].x<<16), __uint_as_float(xv[i][j].x&0xffff0000u), __uint_as_float(xv[i][j].y<<16), __uint_as_float(xv[i][j].y&0xffff0000u)};
      float o[4];
      #pragma unroll
      for(int e=0;e<4;e++){ float m = mm[e]+bf[e];
        float gt = m * __builtin_amdgcn_rcpf(1.f + __builtin_amdgcn_exp2f(-1.44269504f*m));
        o[e] = xf[e] + sv[e]*gt; }
      *(u32x2*)(out + (size_t)(mt*128 + 32*sp + 16*j + fr)*DM + col) = (u32x2){pk_bf16(o[0],o[1]), pk_bf16(o[2],o[3])};
    } } }
  STAMP(1,6);
  if(bid==0 && wid==7){
    #pragma unroll
    for(int i=0;i<4;i++) flag_wait(ctl, 8192 + (i*64+lane)*8, true, tag);
    if(lane==0) flag_set(ctl, 0, tag);
  }
}

extern "C" void kernel_launch(void* const* d_in, const int* in_sizes, int n_in,
                              void* d_out, int out_size, void* d_ws, size_t ws_size,
                              hipStream_t stream) {
  const u16* x=(const u16*)d_in[0]; const u16* cr=(const u16*)d_in[1]; const u16* ci=(const u16*)d_in[2];
  const u16* wre=(const u16*)d_in[3]; const u16* wim=(const u16*)d_in[4];
  const u16* w1=(const u16*)d_in[6]; const u16* b1=(const u16*)d_in[7]; const u16* w2=(const u16*)d_in[8]; const u16* b2=(const u16*)d_in[9];
  char* ws=(char*)d_ws;
  char* himg = ws; char* abimg = ws + (20<<20); char* w2img = ws + (26<<20);
  float* slab=(float*)(ws+(32<<20)); unsigned* cnt=(unsigned*)(ws+(52<<20));
  unsigned long long* stamps = (unsigned long long*)(ws+(128<<20));
  (void)hipFuncSetAttribute((const void*)fused_kernel, hipFuncAttributeMaxDynamicSharedMemorySize, K1_LDS);
  fused_kernel<<<256,512,K1_LDS,stream>>>(x,cr,ci,wre,wim,w1,b1,w2,b2,himg,abimg,w2img,slab,(char*)cnt,(u16*)d_out,stamps);
}
```

```cpp
#define STAMPS
#define STAMPW 1
#include <hip/hip_runtime.h>
#include <hip/hip_bf16.h>
typedef unsigned short u16;
typedef short bf16x8 __attribute__((ext_vector_type(8)));
typedef short s16x4 __attribute__((ext_vector_type(4)));
typedef float f32x4 __attribute__((ext_vector_type(4)));
typedef float f32x2 __attribute__((ext_vector_type(2)));
typedef float f32x16 __attribute__((ext_vector_type(16)));
typedef unsigned u32x4 __attribute__((ext_vector_type(4)));
typedef unsigned u32x2 __attribute__((ext_vector_type(2)));
typedef __bf16 bf2 __attribute__((ext_vector_type(2)));
#define LAS __attribute__((address_space(3)))
typedef LAS s16x4* lds_s16x4;
typedef LAS void* lds_vp;
typedef __amdgpu_buffer_rsrc_t rsrc_t;
#define DI __device__ __forceinline__
DI float bf2f(u16 v){ return __uint_as_float(((unsigned)v)<<16); }
DI u16 f2bf(float f){ unsigned u=__float_as_uint(f); u += 0x7fffu + ((u>>16)&1u); return (u16)(u>>16); }
DI unsigned pk_bf16(float lo, float hi){ bf2 v; v[0]=(__bf16)lo; v[1]=(__bf16)hi; return __builtin_bit_cast(unsigned,v); }
DI float opq(float x){ asm("" : "+v"(x)); return x; }
DI rsrc_t mk_rsrc(const void* p){ return __builtin_amdgcn_make_buffer_rsrc((void*)p, 0, 0x7fffffff, 0x00020000); }
#define ROT 0
#define PRIO 1
#define K2BD 5
#define CPF 1
#define PSLEEP 2
#define DEEP 0
#define ROTF ((mb + 3*nb + (nb/3)) % 9)
#define K2WM 12
#define K2WS 8
#define K2RHO 0
#define K2MIX 0
#define FSLEEP 4
#define SPAREWAIT 0
#define PFD 3
constexpr int SEQ=2048, DM=576, FF=4608;

#define STAMP(K,i) do{ if(lane==0 && (wid==STAMPW)) stamps[((K)*256+blockIdx.x)*16+(i)] = __builtin_amdgcn_s_memrealtime(); }while(0)
#define SB() __builtin_amdgcn_sched_barrier(0)
#define BARRIER() do{ SB(); asm volatile("":::"memory"); __builtin_amdgcn_s_barrier(); asm volatile("":::"memory"); SB(); }while(0)
#define WAITV(n) asm volatile("s_waitcnt vmcnt(%0)"::"n"(n):"memory")
#define WAITL0() asm volatile("s_waitcnt lgkmcnt(0)":::"memory")

DI void dma16(rsrc_t r, unsigned lds_addr, unsigned voff, unsigned soff){
  asm volatile("s_mov_b32 m0, %0\n\ts_nop 4\n\tbuffer_load_dwordx4 %1, %2, %3 offen lds"
               :: "s"(lds_addr), "v"(voff), "s"(r), "s"(soff) : "memory");
}

template<int LOFF, int SOFF>
DI void dma16i(rsrc_t r, unsigned lds_base, unsigned voff){
  unsigned tmp;
  asm volatile("s_add_u32 m0, %1, %2\n\ts_mov_b32 %0, %3\n\ts_nop 3\n\tbuffer_load_dwordx4 %4, %5, %0 offen lds"
               : "=&s"(tmp) : "s"(lds_base), "i"(LOFF), "i"(SOFF), "v"(voff), "s"(r) : "memory", "scc");
}
template<int N> struct IC { static constexpr int v = N; };
template<int B, int E, class Fn> DI void static_for(Fn&& f){ if constexpr (B<E){ f(IC<B>{}); static_for<B+1,E>(f); } }
constexpr int K1_U = 73728;
constexpr int K1_STAT = 2*K1_U;
constexpr int K1_B1   = K1_STAT + 2048;
constexpr int K1_CSP  = K1_B1 + 1536;
constexpr int K1_DUM  = K1_CSP + 2048;
constexpr int K1_TAG  = K1_DUM + 2048;
constexpr int K1_W2S  = K1_TAG + 256;
constexpr int K1_LDS  = K1_W2S + 8192;

template<int LOFF>
DI void dma16f(rsrc_t r, unsigned lds_base, unsigned voff, unsigned soff){
  asm volatile("s_add_u32 m0, %0, %1\n\ts_nop 3\n\tbuffer_load_dwordx4 %2, %3, %4 offen lds"
               :: "s"(lds_base), "i"(LOFF), "v"(voff), "s"(r), "s"(soff) : "memory", "scc");
}
typedef unsigned long long u64;
DI void inbox_fill(const void* slab, int tq, int wid, int lane){
  const u32x4 ff = {0xffffffffu,0xffffffffu,0xffffffffu,0xffffffffu};
  const rsrc_t rq = mk_rsrc(slab);
  #pragma unroll
  for(int i=0;i<4;i++){ const int p = wid + 8*i; if(p<27){ const int k3 = p/9, n = p - k3*9, src = k3 + (k3>=(tq&3) ? 1 : 0);
      __builtin_amdgcn_raw_buffer_store_b128(ff, rq, (unsigned)((tq*4 + src)*9216 + n*1024) + lane*16, 0, 16); } }
}
DI void ctl_fetch(const char* ctl, unsigned lds_dst, int lane){
  const rsrc_t rb = mk_rsrc(ctl);
  asm volatile("s_mov_b32 m0, %0\n\ts_nop 4\n\tbuffer_load_dword %1, %2, 0 offen sc1 lds" :: "s"(lds_dst), "v"(lane*4), "s"(rb) : "memory");
}
DI void flag_set(char* ctl, unsigned off, u64 tag){ __hip_atomic_store((u64*)(ctl + off), tag, __ATOMIC_RELAXED, __HIP_MEMORY_SCOPE_AGENT); }
DI void flag_wait(const char* ctl, unsigned off, bool need, u64 tag){
  const rsrc_t rb = mk_rsrc(ctl);
  for(int spins=0; spins<(1<<20); spins++){
    const u32x2 v = __builtin_amdgcn_raw_buffer_load_b64(rb, off, 0, 16);
    const bool ok = !need || (v[0]==(unsigned)tag && v[1]==(unsigned)(tag>>32));
    if(__all(ok)) break;
    __builtin_amdgcn_s_sleep(FSLEEP);
  }
}
DI int wg_of(int mb, int nb){ const int L = (mb>>3)*120 + nb*8 + (mb&7); const int xc = L/30; return (L - xc*30)*8 + xc; }
DI void touch_lines(const u16* p, unsigned lds_dummy, int lane){
  const rsrc_t rb = mk_rsrc(p);
  asm volatile("s_mov_b32 m0, %0\n\ts_nop 4\n\tbuffer_load_dword %1, %2, 0 offen lds" :: "s"(lds_dummy), "v"(lane*128), "s"(rb) : "memory");
}
template<bool AB, int fb>
DI void k1_body(char* smem, const u16* x, const u16* wre, const u16* wim, const u16* w1, const u16* b1, const u16* w2,
                char* himg, char* abimg, char* w2img, int mb, int nb, int L, int wid, int lane, unsigned long long* stamps, const u16* cpf, const void* slab, char* ctl, int ibx){
  const unsigned lbase = (unsigned)(size_t)(LAS char*)smem;
  const int w4 = wid&3;
  const int r = lane&31, hh = lane>>5;
  constexpr int WROW = AB ? 1152 : FF*2;
  constexpr int WKT  = AB ? 128 : 64*FF*2;
  const rsrc_t rX = mk_rsrc(x + (size_t)mb*128*DM);
  const unsigned voR0 = (lane>>3)*1152 + (((lane&7) ^ (lane>>4))*16);
  const unsigned voW0 = AB ? voR0 : (unsigned)((lane>>3)*(FF*2) + (((lane&7) ^ (((lane>>4)&1)<<2))*16));
  auto wrsrc = [&](int u)->rsrc_t {
    const int uc = u<=5 ? u : 5; const u16* p;
    if constexpr (AB){ int ec = (nb-12)*384 + uc*64; p = ec>=576 ? wim + (size_t)(ec-576)*DM : wre + (size_t)ec*DM; }
    else p = w1 + nb*384 + uc*64;
    return __builtin_amdgcn_make_buffer_rsrc((void*)p, 0, u<=5 ? 0x7fffffff : 0, 0x00020000);
  };
  if constexpr (!AB && fb==0){ if(wid<3){
    const rsrc_t rb = mk_rsrc(b1 + nb*384 + wid*128);
    asm volatile("s_mov_b32 m0, %0\n\ts_nop 4\n\tbuffer_load_dword %1, %2, 0 offen lds" :: "s"(lbase + K1_B1 + wid*256), "v"(lane*4), "s"(rb) : "memory"); } }
  if constexpr (fb==0){ if(wid==3) ctl_fetch(ctl, lbase + K1_TAG, lane); }
  const unsigned voXa = voR0 + wid*16*1152, voXb = (voR0^64) + (wid*16+8)*1152;
  const unsigned voS = (AB ? (voW0 ^ ((wid&1)<<6)) : voW0) + wid*8*WROW;
  const unsigned lbX = lbase + wid*2048, lbS = lbase + wid*1024;
  unsigned kto[9];
  { const int rho = ROTF;
    #pragma unroll
    for(int i=0;i<9;i++){ int t = i + rho; if(t>=9) t -= 9; kto[i] = t; } }
  #define DMAX(kt) do{ dma16f<((3*(kt))%18)*8192>(rX, lbX, voXa, kto[kt]*128); dma16f<((3*(kt))%18)*8192+1024>(rX, lbX, voXb, kto[kt]*128); }while(0)
  #define DMAW(rw,slot,kt) dma16f<(slot)*8192>(rw, lbS, voS, kto[kt]*WKT)
  const rsrc_t rw0 = wrsrc(0), rw1 = wrsrc(1), rw2 = wrsrc(2);
  DMAX(0); DMAW(rw0,2,0);
  DMAX(1); DMAW(rw0,5,1); DMAX(2); DMAW(rw0,8,2);

  unsigned xoff[4];
  unsigned woff[4];
  #pragma unroll
  for(int sp=0;sp<4;sp++){
    int s = sp ^ w4;
    int row = 32*w4 + r;
    xoff[sp] = lbase + row*128 + (((2*s+hh) ^ ((row>>1)&7))<<4);
    if constexpr (AB){ int wr = 32*fb + r; woff[sp] = wr*128 + (((2*s+hh) ^ ((wr>>1)&7))<<4); }
    else { int q=(lane&15)>>2, p=lane&3, blk=(lane>>4)&1; int wr = 16*s + 8*hh + q;
           woff[sp] = wr*128 + ((64*fb + 32*blk + 8*p) ^ (((q>>1)&1)<<6)); }
    woff[sp] += lbase;
  }
  auto wadv = [&](int d){
    #pragma unroll
    for(int sp=0;sp<4;sp++){ woff[sp] += d; asm volatile("" : "+v"(woff[sp])); } };
  auto ldW = [&](int off, int sp)->bf16x8 {
    LAS char* p = (LAS char*)(woff[sp]) + off;
    if constexpr (AB){ return *(const LAS bf16x8*)p; }
    else { s16x4 lo = __builtin_amdgcn_ds_read_tr16_b64_v4i16((lds_s16x4)p);
           s16x4 hi = __builtin_amdgcn_ds_read_tr16_b64_v4i16((lds_s16x4)(p + 512));
           return __builtin_shufflevector(lo,hi,0,1,2,3,4,5,6,7); }
  };
  const short selv = ((lane&15) == ((lane>>4)&1)) ? (short)0x3f80 : (short)0;
  const bf16x8 sel = {selv,selv,selv,selv,selv,selv,selv,selv};

  bf16x8 X[36];
  f32x16 P, Q;
  f32x4 cs = {0.f,0.f,0.f,0.f}, sx = {0.f,0.f,0.f,0.f};
  #pragma unroll
  for(int i=0;i<16;i++){ P[i]=0.f; Q[i]=0.f; }
  constexpr int PD = PFD;
  bf16x8 pw[PD+1];
  STAMP(0,1);
  #define TW(i)
  TW(0);
  if constexpr (fb==1) __builtin_amdgcn_s_setprio(1);
  bf16x8 wf[2][4];
  auto smma = [&](auto KT, auto RDC){
    constexpr int kt = decltype(KT)::v; constexpr bool RD = decltype(RDC)::v;
    #pragma unroll
    for(int sp=0;sp<4;sp++){
      P = __builtin_amdgcn_mfma_f32_32x32x16_bf16(wf[kt&1][sp], X[kt*4+sp], P, 0,0,0);
      if constexpr (RD){ SB(); X[(kt+1)*4+sp] = *(const LAS bf16x8*)((LAS char*)(xoff[sp]) + (((kt+1)&1) ? 24576 : 0)); wf[(kt+1)&1][sp] = ldW(16384,sp); SB(); }
      if(sp==0) cs = __builtin_amdgcn_mfma_f32_16x16x32_bf16(sel, wf[kt&1][0], cs, 0,0,0);
      if((sp>>1)==fb){ Q = __builtin_amdgcn_mfma_f32_32x32x16_bf16(X[kt*4+sp], X[kt*4+sp], Q, 0,0,0);
                       sx = __builtin_amdgcn_mfma_f32_16x16x32_bf16(sel, X[kt*4+sp], sx, 0,0,0); }
    }
    asm volatile("" : "+v"(Q));
    asm volatile("" : "+v"(P), "+v"(sx), "+v"(cs));
  };
  auto sstep = [&](auto KT){
    constexpr int kt = decltype(KT)::v;
    WAITV(6);
    WAITL0();
    BARRIER();
    TW(1+kt);
    auto lds = [&](){
      #pragma unroll
      for(int sp=0;sp<4;sp++){ X[kt*4+sp] = *(const LAS bf16x8*)((LAS char*)(xoff[sp]) + ((kt&1) ? 24576 : 0)); wf[kt&1][sp] = ldW(16384,sp); }
    };
    auto dmas = [&](){
      if constexpr (kt<=5){ DMAX(kt+3); DMAW(rw0,(3*(kt+3)+2)%18,kt+3); }
      else { DMAW(rw1,9+3*(kt-6),3*(kt-6)); DMAW(rw1,10+3*(kt-6),3*(kt-6)+1); DMAW(rw1,11+3*(kt-6),3*(kt-6)+2); }
    };
    if constexpr (kt==0){ lds(); SB(); dmas(); }
    else if constexpr (fb==0){ dmas(); SB(); lds(); SB(); smma(IC<kt-1>{}, IC<0>{}); }
    else     { smma(IC<kt-1>{}, IC<1>{}); SB(); dmas(); }
    wadv(kt==5 ? -5*24576 : 24576);
    if(kt&1){
      #pragma unroll
      for(int sp=0;sp<4;sp++){ xoff[sp] += (kt==5 ? -4*24576 : 2*24576); asm volatile("" : "+v"(xoff[sp])); } }
  };
  sstep(IC<0>{}); sstep(IC<1>{}); sstep(IC<2>{}); sstep(IC<3>{}); sstep(IC<4>{}); sstep(IC<5>{}); sstep(IC<6>{}); sstep(IC<7>{}); sstep(IC<8>{});
  smma(IC<8>{}, IC<0>{});
  if constexpr (fb==1) __builtin_amdgcn_s_setprio(0);
  auto put_cs = [&](int par){
    if(lane<16){ float* d = (float*)(smem + K1_CSP + par*1024 + wid*128 + lane*4); d[0] = cs[0]; d[16] = cs[1]; }
  };
  put_cs(0);
  if(lane<16){ float* d = (float*)(smem + K1_STAT + fb*512 + (32*w4+lane)*4); d[0] = sx[0]; d[16] = sx[1]; }
  { const int dr = (r&3) + 4*(r>>3); float dg = Q[0];
    #pragma unroll
    for(int i=1;i<16;i++) dg = (dr==i) ? Q[i] : dg;
    if(hh == ((r>>2)&1)) *(float*)(smem + K1_STAT + 1024 + fb*512 + (32*w4+r)*4) = dg; }
  WAITV(0); WAITL0();
  BARRIER();
  STAMP(0,2);
  #pragma unroll
  for(int i=0;i<PD;i++) pw[i] = ldW(0,i);
  float rr, irr, K0, K1c; bf16x8 Bext;
  { const float* sp_ = (const float*)(smem + K1_STAT + (32*w4+r)*4); const float s1 = sp_[0] + sp_[128], s2 = sp_[256] + sp_[384];
    float mu = s1*(1.f/DM); float var = fmaxf(s2*(1.f/DM) - mu*mu, 0.f);
    float sd = __builtin_amdgcn_sqrtf(var + 1e-5f);
    irr = sd;
    if constexpr (!AB){ float v2 = var*__builtin_amdgcn_rcpf(var + 1e-5f); irr = sd*__builtin_amdgcn_sqrtf(v2 + 1e-5f); }
    rr = __builtin_amdgcn_rcpf(irr);
    K0 = -2.30220820f*rr; K1c = -0.10294324f*rr*rr*rr;
    unsigned i0 = pk_bf16(irr,irr) & 0xffff0000u; unsigned m0 = pk_bf16(-mu,-mu) & 0xffff0000u;
    unsigned wi = pk_bf16(irr, irr - __uint_as_float(i0)), wm = pk_bf16(-mu, -mu - __uint_as_float(m0));
    u32x4 bx = {wi, wm, wm, 0u};
    if(hh) bx = (u32x4){0u,0u,0u,0u};
    Bext = __builtin_bit_cast(bf16x8, bx); }
  const rsrc_t rH = mk_rsrc(AB ? abimg : himg);

  float cp4[4]; unsigned bw = 0; float to[2]; u32x2 pk[2];
  unsigned obase; float a_r = rr; int ce0=0, ri=0;
  auto epi_load = [&](int u){
    if constexpr (AB){ int ec = (nb-12)*384 + u*64; ri = ec>=576; ce0 = ((ec - ri*576)>>3) + 4*fb; a_r = ri ? -rr : rr; obase = hh*512 + ((r ^ (4*hh))*16); }
    else { obase = (unsigned)(((mb*72 + nb*6 + u)*4 + w4)*4096 + fb*2048) + lane*16;
           bw = *(const u16*)(smem + K1_B1 + u*128 + fb*64 + r*2); }
    const float* cp = (const float*)(smem + K1_CSP + (u&1)*1024 + fb*512);
    cp4[0] = cp[r]; cp4[1] = cp[32+r]; cp4[2] = cp[64+r]; cp4[3] = cp[96+r];
  };
  auto epi_fix = [&](f32x16& O){
    float c = opq(opq(cp4[0]+cp4[1]) + opq(cp4[2]+cp4[3]));
    unsigned c0 = pk_bf16(c,c); float c1 = c - __uint_as_float(c0 & 0xffff0000u);
    u32x4 ax = {bw*0x10001u, c0, pk_bf16(c1,c1), 0u};
    O = __builtin_amdgcn_mfma_f32_32x32x16_bf16(__builtin_bit_cast(bf16x8,ax), Bext, O, 0,0,0);
  };
  float tq[3]; u32x4 vst; unsigned ost;
  auto epi_q = [&](const f32x16& O, int e, int stg){
    float& t = tq[e%3];
    if constexpr (AB){
      if(stg==3) to[e&1] = opq(a_r*O[e]);
    } else {
      const float v = O[e];
      if(stg==0) t = opq(fmaf(opq(v*v), K1c, K0));
      if(stg==1) t = opq(__builtin_amdgcn_exp2f(opq(v*t)));
      if(stg==2) t = opq(__builtin_amdgcn_rcpf(opq(fmaf(t, irr, irr))));
      if(stg==3) to[e&1] = opq(v*t);
    }
    if(stg==3){
      if(e&1) pk[(e>>2)&1][(e>>1)&1] = pk_bf16(to[0],to[1]);
      if((e&7)==7){
        auto s0 = __builtin_amdgcn_permlane32_swap(pk[0][0], pk[1][0], false, false);
        auto s1 = __builtin_amdgcn_permlane32_swap(pk[0][1], pk[1][1], false, false);
        u32x4 v = {s0[0], s1[0], s0[1], s1[1]};
        unsigned so;
        if constexpr (AB){ int ce = ce0 + (e>>3)*2; int nt = ce/18, cin = ce - nt*18; so = (unsigned)((((ri*4+nt)*64 + mb*4 + w4)*18 + cin)*512); }
        else so = (e>>3)*1024;
        vst = v; ost = obase + so;
      }
    }
  };
  auto epi_store = [&](){ __builtin_amdgcn_raw_buffer_store_b128(vst, rH, ost, 0, 16); };
  auto epi_step = [&](const f32x16& O, int kk){
    if(kk<4) return;
    const int e1 = (kk-2)>>1, e0 = e1-1;
    if(kk<8) epi_q(O,0,kk-4);
    if(e0>=1) epi_q(O,e0,kk-2*e0-2);
    if(e1<=15) epi_q(O,e1,kk-2*e1-2);
  };
  #define TS(i)
  rsrc_t rwa = rw2, rwb = wrsrc(3);
  auto interval = [&](f32x16& N, f32x16& O, int u, auto ODDC){
    constexpr int ODD = decltype(ODDC)::v & 1; constexpr bool FIRST = decltype(ODDC)::v == 3;
    TS(0);
    epi_load(u-1);
    const int rli = (L>=120 ? L-120 : L) < 108 ? (L>=120 ? L-120 : L) : 107, rcb = rli/12, rkb = (L>=120 ? 72 : 0) + (rli - rcb*12)*6 + fb;
    if(!FIRST && u<=4){
      const u32x4 v = *(const LAS u32x4*)((LAS char*)(lbase + K1_W2S + fb*4096 + r*128 + (((2*w4+hh) ^ ((r>>1)&7))<<4)));
      const int ce = rcb*8 + 2*w4, nt_ = ce/18, cin = ce - nt_*18;
      __builtin_amdgcn_raw_buffer_store_b128(v, mk_rsrc(w2img), hh*512 + ((r ^ (4*hh))*16) + ((nt_*144 + rkb + 2*(u-2))*18 + cin)*512, 0, 16);
    }
    if(fb==0){ if(u==w4+1) touch_lines(cpf, lbase + K1_DUM + wid*256, lane); }
    static_for<0,36>([&](auto KKC){
      constexpr int kk = decltype(KKC)::v;
      constexpr int kp = kk+PD;
      if(kp==20) wadv(5*8192);
      if(kp==36) wadv(ODD ? -14*8192 : 4*8192);
      pw[kp%(PD+1)] = ldW(kp<20 ? (kp>>2)*8192 : (kp<36 ? ((kp-20)>>2)*8192 : 0), kp&3);
      if constexpr (FIRST){
        if(kk==0+fb) dma16f<0*8192>(rwa,lbS,voS,kto[0]*WKT);
        if(kk==2+fb) dma16f<1*8192>(rwa,lbS,voS,kto[1]*WKT);
        if(kk==4+fb) dma16f<2*8192>(rwa,lbS,voS,kto[2]*WKT);
        if(kk==6+fb) dma16f<3*8192>(rwa,lbS,voS,kto[3]*WKT);
        if(kk==8+fb) dma16f<4*8192>(rwa,lbS,voS,kto[4]*WKT);
        if(kk==10+fb) dma16f<5*8192>(rwa,lbS,voS,kto[5]*WKT);
        if(kk==12+fb) dma16f<6*8192>(rwa,lbS,voS,kto[6]*WKT);
        if(kk==14+fb) dma16f<7*8192>(rwa,lbS,voS,kto[7]*WKT);
        if(kk==16+fb) dma16f<8*8192>(rwa,lbS,voS,kto[8]*WKT);
      } else {
      if(kk==1+2*fb) dma16f<(1-ODD)*K1_U+5*8192>(rwa,lbS,voS,kto[5]*WKT);
      if(kk==5+2*fb) dma16f<(1-ODD)*K1_U+6*8192>(rwa,lbS,voS,kto[6]*WKT);
      if(kk==9+2*fb) dma16f<(1-ODD)*K1_U+7*8192>(rwa,lbS,voS,kto[7]*WKT);
      if(kk==13+2*fb) dma16f<(1-ODD)*K1_U+8*8192>(rwa,lbS,voS,kto[8]*WKT);
      }
      if(kk==21+fb ) dma16f<ODD*K1_U+0*8192>(rwb,lbS,voS,kto[0]*WKT);
      if(kk==24+fb ) dma16f<ODD*K1_U+1*8192>(rwb,lbS,voS,kto[1]*WKT);
      if(kk==27+fb ) dma16f<ODD*K1_U+2*8192>(rwb,lbS,voS,kto[2]*WKT);
      if(kk==30+fb ) dma16f<ODD*K1_U+3*8192>(rwb,lbS,voS,kto[3]*WKT);
      if(kk==32+fb ) dma16f<ODD*K1_U+4*8192>(rwb,lbS,voS,kto[4]*WKT);
      SB();
      if(kk==0){ f32x16 z;
        #pragma unroll
        for(int i=0;i<16;i++) z[i]=0.f;
        N = __builtin_amdgcn_mfma_f32_32x32x16_bf16(pw[0], X[0], z, 0,0,0);
        cs = __builtin_amdgcn_mfma_f32_16x16x32_bf16(sel, pw[0], (f32x4){0.f,0.f,0.f,0.f}, 0,0,0); }
      else { N = __builtin_amdgcn_mfma_f32_32x32x16_bf16(pw[kk%(PD+1)], X[kk], N, 0,0,0);
        if((kk&3)==0) cs = __builtin_amdgcn_mfma_f32_16x16x32_bf16(sel, pw[kk%(PD+1)], cs, 0,0,0);
      }
      if(kk==2) epi_fix(O);
      epi_step(O, kk);
      if(kk==20) epi_store();
      if(kk==0 && !FIRST) epi_store();
      if(kk==34){ put_cs(ODD); rwa = rwb; rwb = wrsrc(u+3); }
      SB();
      if(kk==19){ TS(1); if constexpr (FIRST) WAITV(8); else WAITV(4); BARRIER(); TS(2);
        if(u<=3) dma16(mk_rsrc(w2), lbase + K1_W2S + wid*1024, ((lane>>3)*1152 + (((lane&7) ^ (lane>>4) ^ ((w4&1)<<2))*16)), (unsigned)(((rkb + 2*(u-1))*32 + w4*8)*1152 + rcb*128));
        if(fb==1){ if(u==(w4<3 ? w4+1 : 3)) touch_lines(cpf, lbase + K1_DUM + wid*256, lane); } }
      if(kk==34 && !ODD){ if(u==4) WAITV(0); }
      if(kk==35){ TS(3); WAITV(5); asm volatile("s_waitcnt lgkmcnt(%0)"::"n"(AB ? 1 : 2):"memory"); BARRIER();
        if constexpr (!ODD && fb==0){ if(u==4 && wid==0){ const u64 tag = *(const u64*)(smem + K1_TAG) + 1; if(lane==0) flag_set(ctl, 4096 + blockIdx.x*8, tag); } } }
    });
    TS(10);
    STAMP(0,2+u);
  };
  interval(Q, P, 1, IC<3>{});
  #pragma unroll 1
  for(int it=1; it<3; it++){
    interval(P, Q, 2*it, IC<0>{});
    interval(Q, P, 2*it+1, IC<1>{});
  }
  epi_store();
  inbox_fill(slab, ibx, wid, lane);
  epi_load(5);
  epi_fix(Q);
  static_for<4,36>([&](auto KKC){ epi_step(Q,decltype(KKC)::v); if(decltype(KKC)::v==19 || decltype(KKC)::v==35) epi_store(); });
  STAMP(0,10);
}

constexpr int K2_AB = 128*128;
constexpr int K2_BB = 64*288;
constexpr int K2_ST = K2_AB + K2_BB;
constexpr int K2_LDS = K2_ST*4;

DI void dma16c(rsrc_t r, unsigned lds_addr, unsigned voff, unsigned soff){
  asm volatile("s_mov_b32 m0, %0\n\ts_nop 4\n\tbuffer_load_dwordx4 %1, %2, %3 offen sc1 lds"
               :: "s"(lds_addr), "v"(voff), "s"(r), "s"(soff) : "memory");
}
template<int NP_B>
DI void k2_loader(char* smem, const char* Abase, bool lin, const char* Bbase, int T, int lw, int lane,
                  const char* ctl, u64 tag, unsigned w0off, bool w0need, unsigned w1off, bool w1need){
  const unsigned lbase = (unsigned)(size_t)(LAS char*)smem;
  const rsrc_t rA = mk_rsrc(Abase), rB = mk_rsrc(Bbase);
  unsigned voA[4];
  #pragma unroll
  for(int p=0;p<4;p++){ int row = (lw*4+p)*8 + (lane>>3); int c = (lane&7) ^ ((row>>1)&7);
    voA[p] = lin ? (unsigned)((lw*4+p)*1024 + lane*16) : (unsigned)(row*(SEQ*2) + c*16); }
  const unsigned astep = lin ? 16384u : 128u;
  const int bp0 = lw<2 ? lw*5 : 10 + (lw-2)*4;
  const unsigned voB = bp0*1024 + lane*16;
  constexpr int P = 4+NP_B;
  auto issue = [&](int st){
    int ks = st;
    if(lin){ const int g = st>=18 ? 1 : 0, q = st - 18*g, p = q/3; ks = p*6 + 3*g + (q - 3*p); }
    unsigned dst = lbase + (st&3)*K2_ST;
    #pragma unroll
    for(int p=0;p<4;p++) dma16c(rA, dst + (lw*4+p)*1024, voA[p], ks*astep);
    #pragma unroll
    for(int p=0;p<NP_B;p++) dma16c(rB, dst + K2_AB + (bp0+p)*1024, voB, ks*K2_BB + p*1024);
  };
  flag_wait(ctl, w0off, w0need, tag);
  issue(0); issue(1); issue(2);
  WAITV(2*P);
  BARRIER();
  for(int t=0;t<T;t++){
    if(t+2<T) WAITV(P); else WAITV(0);
    BARRIER();
    if(t==15) flag_wait(ctl, w1off, w1need, tag);
    if(t+3<T) issue(t+3);
  }
}

__global__ __launch_bounds__(512,2) void fused_kernel(const u16* __restrict__ x, const u16* __restrict__ cr, const u16* __restrict__ ci,
    const u16* __restrict__ wre, const u16* __restrict__ wim, const u16* __restrict__ w1, const u16* __restrict__ b1,
    const u16* __restrict__ w2, const u16* __restrict__ b2,
    char* himg, char* abimg, char* w2img, float* slab, char* ctl, u16* out, unsigned long long* stamps){
  extern __shared__ __attribute__((aligned(16))) char smem[];
  const int bid = blockIdx.x; const int xcd = bid&7, idx = bid>>3;
  const int wid = __builtin_amdgcn_readfirstlane(threadIdx.x>>6), lane = threadIdx.x&63;
  const int sp = (0x11330022 >> (4*xcd)) & 3;
  const int mt = (xcd&1)*8 + (idx>>2), nt = idx&3;
  const int tile = mt*4+nt;
  STAMP(0,0);
  const u16* cpf = (bid<128 ? cr : ci) + (size_t)((bid&127)*512 + wid*64)*64;
  if(idx>=30){
    if(wid==3) ctl_fetch(ctl, (unsigned)(size_t)(LAS char*)smem + K1_TAG, lane);
    touch_lines(cpf, (unsigned)(size_t)(LAS char*)smem + wid*256, lane);
    inbox_fill(slab, tile*4+sp, wid, lane);
    STAMP(0,11);
  } else {
  const int L = xcd*30+idx; const int mh = L/120, rem = L - mh*120; const int nb = rem>>3, mb = mh*8 + (rem&7);
  if(nb<12){ if(wid<4) k1_body<false,0>(smem,x,wre,wim,w1,b1,w2,himg,abimg,w2img,mb,nb,L,wid,lane,stamps,cpf,slab,ctl,tile*4+sp);
             else      k1_body<false,1>(smem,x,wre,wim,w1,b1,w2,himg,abimg,w2img,mb,nb,L,wid,lane,stamps,cpf,slab,ctl,tile*4+sp); }
  else     { if(wid<4) k1_body<true ,0>(smem,x,wre,wim,w1,b1,w2,himg,abimg,w2img,mb,nb,L,wid,lane,stamps,cpf,slab,ctl,tile*4+sp);
             else      k1_body<true ,1>(smem,x,wre,wim,w1,b1,w2,himg,abimg,w2img,mb,nb,L,wid,lane,stamps,cpf,slab,ctl,tile*4+sp); }
  }
  WAITV(0); WAITL0();
  BARRIER();
  const u64 tag = *(const u64*)(smem + K1_TAG) + 1;
  if(threadIdx.x==0) flag_set(ctl, 8192 + bid*8, tag);
  const int T = sp<2 ? 36 : 32;
  f32x4 acc[4][9];
  STAMP(1,0);
  if(wid>=4){
    const char* Abase; const char* Bbase;
    if(sp<2){ Abase = himg + (size_t)(mt*72 + sp*36)*16384; Bbase = w2img + (size_t)(nt*144 + sp*72)*9216; }
    else { Abase = (const char*)((sp==2?cr:ci) + (size_t)mt*128*SEQ); Bbase = abimg + (size_t)(((sp-2)*4+nt)*64)*9216; }
    unsigned w0off, w1off = 8192 + (idx*8 + ((0x4062 >> (4*(lane&3))) & 7) + (xcd&1))*8; bool w0need, w1need = (lane>>2)==2;
    if(sp<2){
      const int pw_ = wg_of(mt, 6*sp + (lane<6 ? lane : 0));
      const int Lw = 120*sp + 24*nt + (lane>=16 && lane<52 ? lane-16 : 0);
      w0need = lane<6 || (lane>=16 && lane<52);
      w0off = 4096 + (lane<16 ? pw_ : (Lw%30)*8 + Lw/30)*8;
      if(lane<6){ w1need = true; w1off = 8192 + pw_*8; }
    } else {
      const int ec = (sp-2)*576 + nt*144 + ((lane&16) ? 143 : 0);
      w0need = lane<32; w0off = 8192 + wg_of(lane&15, 12 + ec/384)*8;
    }
    if(wid<6) k2_loader<5>(smem, Abase, sp<2, Bbase, T, wid-4, lane, ctl, tag, w0off, w0need, w1off, w1need);
    else      k2_loader<4>(smem, Abase, sp<2, Bbase, T, wid-4, lane, ctl, tag, w0off, w0need, w1off, w1need);
  } else {
    const int mi = wid>>1, ki = wid&1;
    const int fr = lane&15, g = lane>>4;
    int aoff0, aoff1;
    if(sp<2){ aoff0 = 2*mi*4096 + (4*ki+g)*512 + fr*16; aoff1 = aoff0 + 256; }
    else { aoff0 = (64*mi + fr)*128 + (((4*ki+g) ^ (fr>>1))*16); aoff1 = aoff0 + 2048; }
    const int q = fr>>2, p = lane&3, ph = p>>1;
    const int boff0 = K2_AB + ki*9216 + ph*512 + (8*g + 4*ph + q)*16 + (p&1)*8;
    const int boff1 = K2_AB + ki*9216 + ph*512 + (8*g + 4*(1-ph) + q)*16 + (p&1)*8;
    #pragma unroll
    for(int m=0;m<4;m++)
      #pragma unroll
      for(int n=0;n<9;n++) acc[m][n] = (f32x4){0.f,0.f,0.f,0.f};
    constexpr int BD = K2BD;
    bf16x8 A0[4], A1[4], B[9];
    auto ldA = [&](bf16x8 (&A)[4], int st){ const char* p0 = smem + (st&3)*K2_ST;
      A[0] = *(const bf16x8*)(p0 + aoff0); A[1] = *(const bf16x8*)(p0 + aoff1);
      A[2] = *(const bf16x8*)(p0 + aoff0 + 4096); A[3] = *(const bf16x8*)(p0 + aoff1 + 4096); };
    auto ldB = [&](int st, int n)->bf16x8 { char* p0 = smem + (st&3)*K2_ST + n*1024;
      s16x4 lo = __builtin_amdgcn_ds_read_tr16_b64_v4i16((lds_s16x4)(p0 + boff0));
      s16x4 hi = __builtin_amdgcn_ds_read_tr16_b64_v4i16((lds_s16x4)(p0 + boff1));
      return __builtin_shufflevector(lo,hi,0,1,2,3,4,5,6,7); };
    BARRIER();
    STAMP(1,1);
    ldA(A0,0);
    #pragma unroll
    for(int n=0;n<BD;n++) B[n]=ldB(0,n);
    s16x4 Bl[9], Bh[9];
    #pragma unroll
    for(int n=0;n<BD;n++){ Bl[n] = __builtin_shufflevector(B[n],B[n],0,1,2,3); Bh[n] = __builtin_shufflevector(B[n],B[n],4,5,6,7); }
    const unsigned lb = (unsigned)(size_t)(LAS char*)smem;
    unsigned aA0[2] = {lb + aoff0, lb + aoff0 + 2*K2_ST}, aA1[2] = {lb + aoff1, lb + aoff1 + 2*K2_ST};
    unsigned aB0[2] = {lb + boff0, lb + boff0 + 2*K2_ST}, aB1[2] = {lb + boff1, lb + boff1 + 2*K2_ST};
    #pragma unroll
    for(int i=0;i<2;i++) asm volatile("" : "+v"(aA0[i]), "+v"(aA1[i]), "+v"(aB0[i]), "+v"(aB1[i]));
    auto step = [&](bf16x8 (&Ac)[4], bf16x8 (&An)[4], auto STC){
      constexpr int st = decltype(STC)::v, sn = (st+1)&3;
      #pragma unroll
      for(int n=0;n<9;n++){
        if(n+BD==9) BARRIER();
        const int np = n+BD<9 ? n+BD : n+BD-9;
        const int sb = n+BD<9 ? st : sn;
        const int ob = (sb&1)*K2_ST + np*1024;
        const bf16x8 Bc = __builtin_shufflevector(Bl[n],Bh[n],0,1,2,3,4,5,6,7);
        #pragma unroll
        for(int m=0;m<4;m++){
          acc[m][n] = __builtin_amdgcn_mfma_f32_16x16x32_bf16(Bc, Ac[m], acc[m][n], 0,0,0);
          SB();
          if(m==0) Bl[np] = __builtin_amdgcn_ds_read_tr16_b64_v4i16((lds_s16x4)((LAS char*)(aB0[sb>>1]) + ob));
          if(m==1) Bh[np] = __builtin_amdgcn_ds_read_tr16_b64_v4i16((lds_s16x4)((LAS char*)(aB1[sb>>1]) + ob));
          if(m==2 && n+BD>=9 && n+BD<=12){ const int a = n+BD-9;
            An[a] = *(const LAS bf16x8*)((LAS char*)((a&1) ? aA1[sn>>1] : aA0[sn>>1]) + (sn&1)*K2_ST + (a>>1)*4096); }
          SB();
        }
      }
    };
    for(int t=0;t<T;t+=4){ step(A0,A1,IC<0>{}); step(A1,A0,IC<1>{}); step(A0,A1,IC<2>{}); step(A1,A0,IC<3>{}); }
  }
  STAMP(1,2);
  BARRIER();
  if(wid<4){
    const int ki = wid&1;
    char* dst = smem + (wid^1)*18432 + lane*16;
    #pragma unroll
    for(int j=0;j<2;j++)
      #pragma unroll
      for(int n=0;n<9;n++){ f32x4 v = ki ? acc[j][n] : acc[2+j][n]; *(f32x4*)(dst + (j*9+n)*1024) = v; }
  }
  __syncthreads();
  STAMP(1,3);
  f32x4* own = (f32x4*)(smem + 4*18432);
  const rsrc_t rS = mk_rsrc(slab);
  if(wid<4){
    const int ki = wid&1;
    const char* src = smem + wid*18432 + lane*16;
    const unsigned so = (unsigned)(((tile*4 + wid)*4 + sp)*9216) + lane*16;
    #pragma unroll
    for(int n=0;n<9;n++){
      f32x4 v0 = (ki ? acc[2][n] : acc[0][n]) + *(const f32x4*)(src + n*1024);
      f32x4 v1 = (ki ? acc[3][n] : acc[1][n]) + *(const f32x4*)(src + (9+n)*1024);
      if(wid==sp){ own[n*64+lane] = v0; own[(9+n)*64+lane] = v1; }
      else { u32x4 w = {pk_bf16(v0[0],v0[1]), pk_bf16(v0[2],v0[3]), pk_bf16(v1[0],v1[1]), pk_bf16(v1[2],v1[3])};
        #pragma unroll
        for(int e=0;e<4;e++) w[e] = min(w[e], 0xfffffffeu);
        __builtin_amdgcn_raw_buffer_store_b128(w, rS, so + n*1024, 0, 16); }
    }
  }
  STAMP(1,4);
  const int fr = lane&15, g = lane>>4;
  const int nu = wid==4 ? 2 : 1;
  u32x2 xv[2][2], bv[2];
  #pragma unroll
  for(int i=0;i<2;i++){ if(i<nu){ const int n = i ? 8 : wid; const int col = nt*144 + 16*n + 4*g;
      #pragma unroll
      for(int j=0;j<2;j++) xv[i][j] = *(const u32x2*)(x + (size_t)(mt*128 + 32*sp + 16*j + fr)*DM + col);
      bv[i] = *(const u32x2*)(b2 + col); } }
  u32x4 pp[2][3];
  __syncthreads();
  { int spins = 0; unsigned done = 0;
    while(true){
      #pragma unroll
      for(int q=0;q<3;q++){ if(!((done>>q)&1)){ const int ss = q + (q>=sp ? 1 : 0);
        #pragma unroll
        for(int i=0;i<2;i++){ if(i<nu){ const int n = i ? 8 : wid;
          pp[i][q] = __builtin_amdgcn_raw_buffer_load_b128(rS, (unsigned)(((tile*4 + sp)*4 + ss)*9216) + n*1024 + lane*16, 0, 16); } } } }
      #pragma unroll
      for(int q=0;q<3;q++){ if(!((done>>q)&1)){ unsigned all = 0;
        #pragma unroll
        for(int i=0;i<2;i++){ if(i<nu) all = max(all, max(max(pp[i][q][0],pp[i][q][1]), max(pp[i][q][2],pp[i][q][3]))); }
        if(__all(all != 0xffffffffu)) done |= 1u<<q; } }
      if(done==7u) break;
      if(++spins > (1<<20)) break;
      __builtin_amdgcn_s_sleep(PSLEEP);
    } }
  STAMP(1,5);
  #pragma unroll
  for(int i=0;i<2;i++){ if(i<nu){ const int n = i ? 8 : wid; const int col = nt*144 + 16*n + 4*g;
    const float bf[4] = {__uint_as_float(bv[i].x<<16), __uint_as_float(bv[i].x&0xffff0000u), __uint_as_float(bv[i].y<<16), __uint_as_float(bv[i].y&0xffff0000u)};
    #pragma unroll
    for(int j=0;j<2;j++){
      const f32x4 ow = own[(j*9+n)*64+lane];
      f32x4 p[3];
      #pragma unroll
      for(int q=0;q<3;q++){ const unsigned a = pp[i][q][2*j], b = pp[i][q][2*j+1];
        p[q] = (f32x4){__uint_as_float(a<<16), __uint_as_float(a&0xffff0000u), __uint_as_float(b<<16), __uint_as_float(b&0xffff0000u)}; }
      f32x4 mm, sv;
      if(sp<2){ mm = ow + p[0]; sv = p[1] + p[2]; }
      else    { mm = p[0] + p[1]; sv = ow + p[2]; }
      const float xf[4] = {__uint_as_float(xv[i][j].x<<16), __uint_as_float(xv[i][j].x&0xffff0000u), __uint_as_float(xv[i][j].y<<16), __uint_as_float(xv[i][j].y&0xffff0000u)};
      float o[4];
      #pragma unroll
      for(int e=0;e<4;e++){ float m = mm[e]+bf[e];
        float gt = m * __builtin_amdgcn_rcpf(1.f + __builtin_amdgcn_exp2f(-1.44269504f*m));
        o[e] = xf[e] + sv[e]*gt; }
      *(u32x2*)(out + (size_t)(mt*128 + 32*sp + 16*j + fr)*DM + col) = (u32x2){pk_bf16(o[0],o[1]), pk_bf16(o[2],o[3])};
    } } }
  STAMP(1,6);
  if(bid==0 && wid==7){
    #pragma unroll
    for(int i=0;i<4;i++) flag_wait(ctl, 8192 + (i*64+lane)*8, true, tag);
    if(lane==0) flag_set(ctl, 0, tag);
  }
}

__global__ void rep_kernel(const unsigned long long* st){
  __shared__ double sh[2];
  if(threadIdx.x<2){
    int k = threadIdx.x? 0:0, s = threadIdx.x? 0:0, m = threadIdx.x? 0:0;
    unsigned long long base = st[(256)*16];
    double mn=1e30,mx=-1e30,av=0; int n=0;
    for(int b=0;b<256;b++){ if(k==0 && s>0 && (b>>3)>=30) continue; double v=(double)(long long)(st[(k*256+b)*16+s]-base); mn=fmin(mn,v); mx=fmax(mx,v); av+=v; n++; }
    sh[threadIdx.x] = m==0?mn:(m==1?mx:av/n);
  }
  __syncthreads();
  double d = sh[1]-sh[0]; if(d<0) d=0; if(d>20000) d=20000;
  unsigned long long t0=__builtin_amdgcn_s_memrealtime();
  while((double)(__builtin_amdgcn_s_memrealtime()-t0) < d*100.0) __builtin_amdgcn_s_sleep(10);
}

extern "C" void kernel_launch(void* const* d_in, const int* in_sizes, int n_in,
                              void* d_out, int out_size, void* d_ws, size_t ws_size,
                              hipStream_t stream) {
  const u16* x=(const u16*)d_in[0]; const u16* cr=(const u16*)d_in[1]; const u16* ci=(const u16*)d_in[2];
  const u16* wre=(const u16*)d_in[3]; const u16* wim=(const u16*)d_in[4];
  const u16* w1=(const u16*)d_in[6]; const u16* b1=(const u16*)d_in[7]; const u16* w2=(const u16*)d_in[8]; const u16* b2=(const u16*)d_in[9];
  char* ws=(char*)d_ws;
  char* himg = ws; char* abimg = ws + (20<<20); char* w2img = ws + (26<<20);
  float* slab=(float*)(ws+(32<<20)); unsigned* cnt=(unsigned*)(ws+(52<<20));
  unsigned long long* stamps = (unsigned long long*)(ws+(128<<20));
  (void)hipFuncSetAttribute((const void*)fused_kernel, hipFuncAttributeMaxDynamicSharedMemorySize, K1_LDS);
  fused_kernel<<<256,512,K1_LDS,stream>>>(x,cr,ci,wre,wim,w1,b1,w2,b2,himg,abimg,w2img,slab,(char*)cnt,(u16*)d_out,stamps);
  rep_kernel<<<1,64,0,stream>>>(stamps);
}
```

```cpp
#include <hip/hip_runtime.h>
#include <hip/hip_bf16.h>
typedef unsigned short u16;
typedef short bf16x8 __attribute__((ext_vector_type(8)));
typedef short s16x4 __attribute__((ext_vector_type(4)));
typedef float f32x4 __attribute__((ext_vector_type(4)));
typedef float f32x2 __attribute__((ext_vector_type(2)));
typedef float f32x16 __attribute__((ext_vector_type(16)));
typedef unsigned u32x4 __attribute__((ext_vector_type(4)));
typedef unsigned u32x2 __attribute__((ext_vector_type(2)));
typedef __bf16 bf2 __attribute__((ext_vector_type(2)));
#define LAS __attribute__((address_space(3)))
typedef LAS s16x4* lds_s16x4;
typedef LAS void* lds_vp;
typedef __amdgpu_buffer_rsrc_t rsrc_t;
#define DI __device__ __forceinline__
DI float bf2f(u16 v){ return __uint_as_float(((unsigned)v)<<16); }
DI u16 f2bf(float f){ unsigned u=__float_as_uint(f); u += 0x7fffu + ((u>>16)&1u); return (u16)(u>>16); }
DI unsigned pk_bf16(float lo, float hi){ bf2 v; v[0]=(__bf16)lo; v[1]=(__bf16)hi; return __builtin_bit_cast(unsigned,v); }
DI float opq(float x){ asm("" : "+v"(x)); return x; }
DI rsrc_t mk_rsrc(const void* p){ return __builtin_amdgcn_make_buffer_rsrc((void*)p, 0, 0x7fffffff, 0x00020000); }
#define ROT 0
#define PRIO 1
#define K2BD 5
#define CPF 1
#define PSLEEP 2
#define DEEP 0
#define ROTF ((mb + 3*nb + (nb/3)) % 9)
#define K2WM 12
#define K2WS 8
#define K2RHO 0
#define K2MIX 0
#define FSLEEP 4
#define SPAREWAIT 0
#define PFD 3
constexpr int SEQ=2048, DM=576, FF=4608;

#define STAMP(K,i)
#define SB() __builtin_amdgcn_sched_barrier(0)
#define BARRIER() do{ SB(); asm volatile("":::"memory"); __builtin_amdgcn_s_barrier(); asm volatile("":::"memory"); SB(); }while(0)
#define WAITV(n) asm volatile("s_waitcnt vmcnt(%0)"::"n"(n):"memory")
#define WAITL0() asm volatile("s_waitcnt lgkmcnt(0)":::"memory")

DI void dma16(rsrc_t r, unsigned lds_addr, unsigned voff, unsigned soff){
  asm volatile("s_mov_b32 m0, %0\n\ts_nop 4\n\tbuffer_load_dwordx4 %1, %2, %3 offen lds"
               :: "s"(lds_addr), "v"(voff), "s"(r), "s"(soff) : "memory");
}

template<int LOFF, int SOFF>
DI void dma16i(rsrc_t r, unsigned lds_base, unsigned voff){
  unsigned tmp;
  asm volatile("s_add_u32 m0, %1, %2\n\ts_mov_b32 %0, %3\n\ts_nop 3\n\tbuffer_load_dwordx4 %4, %5, %0 offen lds"
               : "=&s"(tmp) : "s"(lds_base), "i"(LOFF), "i"(SOFF), "v"(voff), "s"(r) : "memory", "scc");
}
template<int N> struct IC { static constexpr int v = N; };
template<int B, int E, class Fn> DI void static_for(Fn&& f){ if constexpr (B<E){ f(IC<B>{}); static_for<B+1,E>(f); } }
constexpr int K1_U = 73728;
constexpr int K1_STAT = 2*K1_U;
constexpr int K1_B1   = K1_STAT + 2048;
constexpr int K1_CSP  = K1_B1 + 1536;
constexpr int K1_DUM  = K1_CSP + 2048;
constexpr int K1_TAG  = K1_DUM + 2048;
constexpr int K1_W2S  = K1_TAG + 256;
constexpr int K1_LDS  = K1_W2S + 8192;

template<int LOFF>
DI void dma16f(rsrc_t r, unsigned lds_base, unsigned voff, unsigned soff){
  asm volatile("s_add_u32 m0, %0, %1\n\ts_nop 3\n\tbuffer_load_dwordx4 %2, %3, %4 offen lds"
               :: "s"(lds_base), "i"(LOFF), "v"(voff), "s"(r), "s"(soff) : "memory", "scc");
}
typedef unsigned long long u64;
DI void inbox_fill(const void* slab, int tq, int wid, int lane){
  const u32x4 ff = {0xffffffffu,0xffffffffu,0xffffffffu,0xffffffffu};
  const rsrc_t rq = mk_rsrc(slab);
  #pragma unroll
  for(int i=0;i<4;i++){ const int p = wid + 8*i; if(p<27){ const int k3 = p/9, n = p - k3*9, src = k3 + (k3>=(tq&3) ? 1 : 0);
      __builtin_amdgcn_raw_buffer_store_b128(ff, rq, (unsigned)((tq*4 + src)*9216 + n*1024) + lane*16, 0, 16); } }
}
DI void ctl_fetch(const char* ctl, unsigned lds_dst, int lane){
  const rsrc_t rb = mk_rsrc(ctl);
  asm volatile("s_mov_b32 m0, %0\n\ts_nop 4\n\tbuffer_load_dword %1, %2, 0 offen sc1 lds" :: "s"(lds_dst), "v"(lane*4), "s"(rb) : "memory");
}
DI void flag_set(char* ctl, unsigned off, u64 tag){ __hip_atomic_store((u64*)(ctl + off), tag, __ATOMIC_RELAXED, __HIP_MEMORY_SCOPE_AGENT); }
DI void flag_wait(const char* ctl, unsigned off, bool need, u64 tag){
  const rsrc_t rb = mk_rsrc(ctl);
  for(int spins=0; spins<(1<<20); spins++){
    const u32x2 v = __builtin_amdgcn_raw_buffer_load_b64(rb, off, 0, 16);
    const bool ok = !need || (v[0]==(unsigned)tag && v[1]==(unsigned)(tag>>32));
    if(__all(ok)) break;
    __builtin_amdgcn_s_sleep(FSLEEP);
  }
}
DI int wg_of(int mb, int nb){ const int L = (mb>>3)*120 + nb*8 + (mb&7); const int xc = L/30; return (L - xc*30)*8 + xc; }
DI void touch_lines(const u16* p, unsigned lds_dummy, int lane){
  const rsrc_t rb = mk_rsrc(p);
  asm volatile("s_mov_b32 m0, %0\n\ts_nop 4\n\tbuffer_load_dword %1, %2, 0 offen lds" :: "s"(lds_dummy), "v"(lane*128), "s"(rb) : "memory");
}
template<bool AB, int fb>
DI void k1_body(char* smem, const u16* x, const u16* wre, const u16* wim, const u16* w1, const u16* b1, const u16* w2,
                char* himg, char* abimg, char* w2img, int mb, int nb, int L, int wid, int lane, unsigned long long* stamps, const u16* cpf, const void* slab, char* ctl, int ibx){
  const unsigned lbase = (unsigned)(size_t)(LAS char*)smem;
  const int w4 = wid&3;
  const int r = lane&31, hh = lane>>5;
  constexpr int WROW = AB ? 1152 : FF*2;
  constexpr int WKT  = AB ? 128 : 64*FF*2;
  const rsrc_t rX = mk_rsrc(x + (size_t)mb*128*DM);
  const unsigned voR0 = (lane>>3)*1152 + (((lane&7) ^ (lane>>4))*16);
  const unsigned voW0 = AB ? voR0 : (unsigned)((lane>>3)*(FF*2) + (((lane&7) ^ (((lane>>4)&1)<<2))*16));
  auto wrsrc = [&](int u)->rsrc_t {
    const int uc = u<=5 ? u : 5; const u16* p;
    if constexpr (AB){ int ec = (nb-12)*384 + uc*64; p = ec>=576 ? wim + (size_t)(ec-576)*DM : wre + (size_t)ec*DM; }
    else p = w1 + nb*384 + uc*64;
    return __builtin_amdgcn_make_buffer_rsrc((void*)p, 0, u<=5 ? 0x7fffffff : 0, 0x00020000);
  };
  if constexpr (!AB && fb==0){ if(wid<3){
    const rsrc_t rb = mk_rsrc(b1 + nb*384 + wid*128);
    asm volatile("s_mov_b32 m0, %0\n\ts_nop 4\n\tbuffer_load_dword %1, %2, 0 offen lds" :: "s"(lbase + K1_B1 + wid*256), "v"(lane*4), "s"(rb) : "memory"); } }
  if constexpr (fb==0){ if(wid==3) ctl_fetch(ctl, lbase + K1_TAG, lane); }
  const unsigned voXa = voR0 + wid*16*1152, voXb = (voR0^64) + (wid*16+8)*1152;
  const unsigned voS = (AB ? (voW0 ^ ((wid&1)<<6)) : voW0) + wid*8*WROW;
  const unsigned lbX = lbase + wid*2048, lbS = lbase + wid*1024;
  unsigned kto[9];
  { const int rho = ROTF;
    #pragma unroll
    for(int i=0;i<9;i++){ int t = i + rho; if(t>=9) t -= 9; kto[i] = t; } }
  #define DMAX(kt) do{ dma16f<((3*(kt))%18)*8192>(rX, lbX, voXa, kto[kt]*128); dma16f<((3*(kt))%18)*8192+1024>(rX, lbX, voXb, kto[kt]*128); }while(0)
  #define DMAW(rw,slot,kt) dma16f<(slot)*8192>(rw, lbS, voS, kto[kt]*WKT)
  const rsrc_t rw0 = wrsrc(0), rw1 = wrsrc(1), rw2 = wrsrc(2);
  DMAX(0); DMAW(rw0,2,0);
  DMAX(1); DMAW(rw0,5,1); DMAX(2); DMAW(rw0,8,2);

  unsigned xoff[4];
  unsigned woff[4];
  #pragma unroll
  for(int sp=0;sp<4;sp++){
    int s = sp ^ w4;
    int row = 32*w4 + r;
    xoff[sp] = lbase + row*128 + (((2*s+hh) ^ ((row>>1)&7))<<4);
    if constexpr (AB){ int wr = 32*fb + r; woff[sp] = wr*128 + (((2*s+hh) ^ ((wr>>1)&7))<<4); }
    else { int q=(lane&15)>>2, p=lane&3, blk=(lane>>4)&1; int wr = 16*s + 8*hh + q;
           woff[sp] = wr*128 + ((64*fb + 32*blk + 8*p) ^ (((q>>1)&1)<<6)); }
    woff[sp] += lbase;
  }
  auto wadv = [&](int d){
    #pragma unroll
    for(int sp=0;sp<4;sp++){ woff[sp] += d; asm volatile("" : "+v"(woff[sp])); } };
  auto ldW = [&](int off, int sp)->bf16x8 {
    LAS char* p = (LAS char*)(woff[sp]) + off;
    if constexpr (AB){ return *(const LAS bf16x8*)p; }
    else { s16x4 lo = __builtin_amdgcn_ds_read_tr16_b64_v4i16((lds_s16x4)p);
           s16x4 hi = __builtin_amdgcn_ds_read_tr16_b64_v4i16((lds_s16x4)(p + 512));
           return __builtin_shufflevector(lo,hi,0,1,2,3,4,5,6,7); }
  };
  const short selv = ((lane&15) == ((lane>>4)&1)) ? (short)0x3f80 : (short)0;
  const bf16x8 sel = {selv,selv,selv,selv,selv,selv,selv,selv};

  bf16x8 X[36];
  f32x16 P, Q;
  f32x4 cs = {0.f,0.f,0.f,0.f}, sx = {0.f,0.f,0.f,0.f};
  #pragma unroll
  for(int i=0;i<16;i++){ P[i]=0.f; Q[i]=0.f; }
  constexpr int PD = PFD;
  bf16x8 pw[PD+1];
  STAMP(0,1);
  #define TW(i)
  TW(0);
  if constexpr (fb==1) __builtin_amdgcn_s_setprio(1);
  bf16x8 wf[2][4];
  auto smma = [&](auto KT, auto RDC){
    constexpr int kt = decltype(KT)::v; constexpr bool RD = decltype(RDC)::v;
    #pragma unroll
    for(int sp=0;sp<4;sp++){
      P = __builtin_amdgcn_mfma_f32_32x32x16_bf16(wf[kt&1][sp], X[kt*4+sp], P, 0,0,0);
      if constexpr (RD){ SB(); X[(kt+1)*4+sp] = *(const LAS bf16x8*)((LAS char*)(xoff[sp]) + (((kt+1)&1) ? 24576 : 0)); wf[(kt+1)&1][sp] = ldW(16384,sp); SB(); }
      if(sp==0) cs = __builtin_amdgcn_mfma_f32_16x16x32_bf16(sel, wf[kt&1][0], cs, 0,0,0);
      if((sp>>1)==fb){ Q = __builtin_amdgcn_mfma_f32_32x32x16_bf16(X[kt*4+sp], X[kt*4+sp], Q, 0,0,0);
                       sx = __builtin_amdgcn_mfma_f32_16x16x32_bf16(sel, X[kt*4+sp], sx, 0,0,0); }
    }
    asm volatile("" : "+v"(Q));
    asm volatile("" : "+v"(P), "+v"(sx), "+v"(cs));
  };
  auto sstep = [&](auto KT){
    constexpr int kt = decltype(KT)::v;
    WAITV(6);
    WAITL0();
    BARRIER();
    TW(1+kt);
    auto lds = [&](){
      #pragma unroll
      for(int sp=0;sp<4;sp++){ X[kt*4+sp] = *(const LAS bf16x8*)((LAS char*)(xoff[sp]) + ((kt&1) ? 24576 : 0)); wf[kt&1][sp] = ldW(16384,sp); }
    };
    auto dmas = [&](){
      if constexpr (kt<=5){ DMAX(kt+3); DMAW(rw0,(3*(kt+3)+2)%18,kt+3); }
      else { DMAW(rw1,9+3*(kt-6),3*(kt-6)); DMAW(rw1,10+3*(kt-6),3*(kt-6)+1); DMAW(rw1,11+3*(kt-6),3*(kt-6)+2); }
    };
    if constexpr (kt==0){ lds(); SB(); dmas(); }
    else if constexpr (fb==0){ dmas(); SB(); lds(); SB(); smma(IC<kt-1>{}, IC<0>{}); }
    else     { smma(IC<kt-1>{}, IC<1>{}); SB(); dmas(); }
    wadv(kt==5 ? -5*24576 : 24576);
    if(kt&1){
      #pragma unroll
      for(int sp=0;sp<4;sp++){ xoff[sp] += (kt==5 ? -4*24576 : 2*24576); asm volatile("" : "+v"(xoff[sp])); } }
  };
  sstep(IC<0>{}); sstep(IC<1>{}); sstep(IC<2>{}); sstep(IC<3>{}); sstep(IC<4>{}); sstep(IC<5>{}); sstep(IC<6>{}); sstep(IC<7>{}); sstep(IC<8>{});
  smma(IC<8>{}, IC<0>{});
  if constexpr (fb==1) __builtin_amdgcn_s_setprio(0);
  auto put_cs = [&](int par){
    if(lane<16){ float* d = (float*)(smem + K1_CSP + par*1024 + wid*128 + lane*4); d[0] = cs[0]; d[16] = cs[1]; }
  };
  put_cs(0);
  if(lane<16){ float* d = (float*)(smem + K1_STAT + fb*512 + (32*w4+lane)*4); d[0] = sx[0]; d[16] = sx[1]; }
  { const int dr = (r&3) + 4*(r>>3); float dg = Q[0];
    #pragma unroll
    for(int i=1;i<16;i++) dg = (dr==i) ? Q[i] : dg;
    if(hh == ((r>>2)&1)) *(float*)(smem + K1_STAT + 1024 + fb*512 + (32*w4+r)*4) = dg; }
  WAITV(0); WAITL0();
  BARRIER();
  STAMP(0,2);
  #pragma unroll
  for(int i=0;i<PD;i++) pw[i] = ldW(0,i);
  float rr, irr, K0, K1c; bf16x8 Bext;
  { const float* sp_ = (const float*)(smem + K1_STAT + (32*w4+r)*4); const float s1 = sp_[0] + sp_[128], s2 = sp_[256] + sp_[384];
    float mu = s1*(1.f/DM); float var = fmaxf(s2*(1.f/DM) - mu*mu, 0.f);
    float sd = __builtin_amdgcn_sqrtf(var + 1e-5f);
    irr = sd;
    if constexpr (!AB){ float v2 = var*__builtin_amdgcn_rcpf(var + 1e-5f); irr = sd*__builtin_amdgcn_sqrtf(v2 + 1e-5f); }
    rr = __builtin_amdgcn_rcpf(irr);
    K0 = -2.30220820f*rr; K1c = -0.10294324f*rr*rr*rr;
    unsigned i0 = pk_bf16(irr,irr) & 0xffff0000u; unsigned m0 = pk_bf16(-mu,-mu) & 0xffff0000u;
    unsigned wi = pk_bf16(irr, irr - __uint_as_float(i0)), wm = pk_bf16(-mu, -mu - __uint_as_float(m0));
    u32x4 bx = {wi, wm, wm, 0u};
    if(hh) bx = (u32x4){0u,0u,0u,0u};
    Bext = __builtin_bit_cast(bf16x8, bx); }
  const rsrc_t rH = mk_rsrc(AB ? abimg : himg);

  float cp4[4]; unsigned bw = 0; float to[2]; u32x2 pk[2];
  unsigned obase; float a_r = rr; int ce0=0, ri=0;
  auto epi_load = [&](int u){
    if constexpr (AB){ int ec = (nb-12)*384 + u*64; ri = ec>=576; ce0 = ((ec - ri*576)>>3) + 4*fb; a_r = ri ? -rr : rr; obase = hh*512 + ((r ^ (4*hh))*16); }
    else { obase = (unsigned)(((mb*72 + nb*6 + u)*4 + w4)*4096 + fb*2048) + lane*16;
           bw = *(const u16*)(smem + K1_B1 + u*128 + fb*64 + r*2); }
    const float* cp = (const float*)(smem + K1_CSP + (u&1)*1024 + fb*512);
    cp4[0] = cp[r]; cp4[1] = cp[32+r]; cp4[2] = cp[64+r]; cp4[3] = cp[96+r];
  };
  auto epi_fix = [&](f32x16& O){
    float c = opq(opq(cp4[0]+cp4[1]) + opq(cp4[2]+cp4[3]));
    unsigned c0 = pk_bf16(c,c); float c1 = c - __uint_as_float(c0 & 0xffff0000u);
    u32x4 ax = {bw*0x10001u, c0, pk_bf16(c1,c1), 0u};
    O = __builtin_amdgcn_mfma_f32_32x32x16_bf16(__builtin_bit_cast(bf16x8,ax), Bext, O, 0,0,0);
  };
  float tq[3]; u32x4 vst; unsigned ost;
  auto epi_q = [&](const f32x16& O, int e, int stg){
    float& t = tq[e%3];
    if constexpr (AB){
      if(stg==3) to[e&1] = opq(a_r*O[e]);
    } else {
      const float v = O[e];
      if(stg==0) t = opq(fmaf(opq(v*v), K1c, K0));
      if(stg==1) t = opq(__builtin_amdgcn_exp2f(opq(v*t)));
      if(stg==2) t = opq(__builtin_amdgcn_rcpf(opq(fmaf(t, irr, irr))));
      if(stg==3) to[e&1] = opq(v*t);
    }
    if(stg==3){
      if(e&1) pk[(e>>2)&1][(e>>1)&1] = pk_bf16(to[0],to[1]);
      if((e&7)==7){
        auto s0 = __builtin_amdgcn_permlane32_swap(pk[0][0], pk[1][0], false, false);
        auto s1 = __builtin_amdgcn_permlane32_swap(pk[0][1], pk[1][1], false, false);
        u32x4 v = {s0[0], s1[0], s0[1], s1[1]};
        unsigned so;
        if constexpr (AB){ int ce = ce0 + (e>>3)*2; int nt = ce/18, cin = ce - nt*18; so = (unsigned)((((ri*4+nt)*64 + mb*4 + w4)*18 + cin)*512); }
        else so = (e>>3)*1024;
        vst = v; ost = obase + so;
      }
    }
  };
  auto epi_store = [&](){ __builtin_amdgcn_raw_buffer_store_b128(vst, rH, ost, 0, 16); };
  auto epi_step = [&](const f32x16& O, int kk){
    if(kk<4) return;
    const int e1 = (kk-2)>>1, e0 = e1-1;
    if(kk<8) epi_q(O,0,kk-4);
    if(e0>=1) epi_q(O,e0,kk-2*e0-2);
    if(e1<=15) epi_q(O,e1,kk-2*e1-2);
  };
  #define TS(i)
  rsrc_t rwa = rw2, rwb = wrsrc(3);
  auto interval = [&](f32x16& N, f32x16& O, int u, auto ODDC){
    constexpr int ODD = decltype(ODDC)::v & 1; constexpr bool FIRST = decltype(ODDC)::v == 3;
    TS(0);
    epi_load(u-1);
    const int rli = (L>=120 ? L-120 : L) < 108 ? (L>=120 ? L-120 : L) : 107, rcb = rli/12, rkb = (L>=120 ? 72 : 0) + (rli - rcb*12)*6 + fb;
    if(!FIRST && u<=4){
      const u32x4 v = *(const LAS u32x4*)((LAS char*)(lbase + K1_W2S + fb*4096 + r*128 + (((2*w4+hh) ^ ((r>>1)&7))<<4)));
      const int ce = rcb*8 + 2*w4, nt_ = ce/18, cin = ce - nt_*18;
      __builtin_amdgcn_raw_buffer_store_b128(v, mk_rsrc(w2img), hh*512 + ((r ^ (4*hh))*16) + ((nt_*144 + rkb + 2*(u-2))*18 + cin)*512, 0, 16);
    }
    if(fb==0){ if(u==w4+1) touch_lines(cpf, lbase + K1_DUM + wid*256, lane); }
    static_for<0,36>([&](auto KKC){
      constexpr int kk = decltype(KKC)::v;
      constexpr int kp = kk+PD;
      if(kp==20) wadv(5*8192);
      if(kp==36) wadv(ODD ? -14*8192 : 4*8192);
      pw[kp%(PD+1)] = ldW(kp<20 ? (kp>>2)*8192 : (kp<36 ? ((kp-20)>>2)*8192 : 0), kp&3);
      if constexpr (FIRST){
        if(kk==0+fb) dma16f<0*8192>(rwa,lbS,voS,kto[0]*WKT);
        if(kk==2+fb) dma16f<1*8192>(rwa,lbS,voS,kto[1]*WKT);
        if(kk==4+fb) dma16f<2*8192>(rwa,lbS,voS,kto[2]*WKT);
        if(kk==6+fb) dma16f<3*8192>(rwa,lbS,voS,kto[3]*WKT);
        if(kk==8+fb) dma16f<4*8192>(rwa,lbS,voS,kto[4]*WKT);
        if(kk==10+fb) dma16f<5*8192>(rwa,lbS,voS,kto[5]*WKT);
        if(kk==12+fb) dma16f<6*8192>(rwa,lbS,voS,kto[6]*WKT);
        if(kk==14+fb) dma16f<7*8192>(rwa,lbS,voS,kto[7]*WKT);
        if(kk==16+fb) dma16f<8*8192>(rwa,lbS,voS,kto[8]*WKT);
      } else {
      if(kk==1+2*fb) dma16f<(1-ODD)*K1_U+5*8192>(rwa,lbS,voS,kto[5]*WKT);
      if(kk==5+2*fb) dma16f<(1-ODD)*K1_U+6*8192>(rwa,lbS,voS,kto[6]*WKT);
      if(kk==9+2*fb) dma16f<(1-ODD)*K1_U+7*8192>(rwa,lbS,voS,kto[7]*WKT);
      if(kk==13+2*fb) dma16f<(1-ODD)*K1_U+8*8192>(rwa,lbS,voS,kto[8]*WKT);
      }
      if(kk==21+fb ) dma16f<ODD*K1_U+0*8192>(rwb,lbS,voS,kto[0]*WKT);
      if(kk==24+fb ) dma16f<ODD*K1_U+1*8192>(rwb,lbS,voS,kto[1]*WKT);
      if(kk==27+fb ) dma16f<ODD*K1_U+2*8192>(rwb,lbS,voS,kto[2]*WKT);
      if(kk==30+fb ) dma16f<ODD*K1_U+3*8192>(rwb,lbS,voS,kto[3]*WKT);
      if(kk==32+fb ) dma16f<ODD*K1_U+4*8192>(rwb,lbS,voS,kto[4]*WKT);
      SB();
      if(kk==0){ f32x16 z;
        #pragma unroll
        for(int i=0;i<16;i++) z[i]=0.f;
        N = __builtin_amdgcn_mfma_f32_32x32x16_bf16(pw[0], X[0], z, 0,0,0);
        cs = __builtin_amdgcn_mfma_f32_16x16x32_bf16(sel, pw[0], (f32x4){0.f,0.f,0.f,0.f}, 0,0,0); }
      else { N = __builtin_amdgcn_mfma_f32_32x32x16_bf16(pw[kk%(PD+1)], X[kk], N, 0,0,0);
        if((kk&3)==0) cs = __builtin_amdgcn_mfma_f32_16x16x32_bf16(sel, pw[kk%(PD+1)], cs, 0,0,0);
      }
      if(kk==2) epi_fix(O);
      epi_step(O, kk);
      if(kk==20) epi_store();
      if(kk==0 && !FIRST) epi_store();
      if(kk==34){ put_cs(ODD); rwa = rwb; rwb = wrsrc(u+3); }
      SB();
      if(kk==19){ TS(1); if constexpr (FIRST) WAITV(8); else WAITV(4); BARRIER(); TS(2);
        if(u<=3) dma16(mk_rsrc(w2), lbase + K1_W2S + wid*1024, ((lane>>3)*1152 + (((lane&7) ^ (lane>>4) ^ ((w4&1)<<2))*16)), (unsigned)(((rkb + 2*(u-1))*32 + w4*8)*1152 + rcb*128));
        if(fb==1){ if(u==(w4<3 ? w4+1 : 3)) touch_lines(cpf, lbase + K1_DUM + wid*256, lane); } }
      if(kk==34 && !ODD){ if(u==4) WAITV(0); }
      if(kk==35){ TS(3); WAITV(5); asm volatile("s_waitcnt lgkmcnt(%0)"::"n"(AB ? 1 : 2):"memory"); BARRIER();
        if constexpr (!ODD && fb==0){ if(u==4 && wid==0){ const u64 tag = *(const u64*)(smem + K1_TAG) + 1; if(lane==0) flag_set(ctl, 4096 + blockIdx.x*8, tag); } } }
    });
    TS(10);
    STAMP(0,2+u);
  };
  interval(Q, P, 1, IC<3>{});
  #pragma unroll 1
  for(int it=1; it<3; it++){
    interval(P, Q, 2*it, IC<0>{});
    interval(Q, P, 2*it+1, IC<1>{});
  }
  epi_store();
  inbox_fill(slab, ibx, wid, lane);
  epi_load(5);
  epi_fix(Q);
  static_for<4,36>([&](auto KKC){ epi_step(Q,decltype(KKC)::v); if(decltype(KKC)::v==19 || decltype(KKC)::v==35) epi_store(); });
  STAMP(0,10);
}

constexpr int K2_AB = 128*128;
constexpr int K2_BB = 64*288;
constexpr int K2_ST = K2_AB + K2_BB;
constexpr int K2_LDS = K2_ST*4;

DI void dma16c(rsrc_t r, unsigned lds_addr, unsigned voff, unsigned soff){
  asm volatile("s_mov_b32 m0, %0\n\ts_nop 4\n\tbuffer_load_dwordx4 %1, %2, %3 offen sc1 lds"
               :: "s"(lds_addr), "v"(voff), "s"(r), "s"(soff) : "memory");
}
template<int NP_B>
DI void k2_loader(char* smem, const char* Abase, bool lin, const char* Bbase, int T, int lw, int lane,
                  const char* ctl, u64 tag, unsigned w0off, bool w0need, unsigned w1off, bool w1need){
  const unsigned lbase = (unsigned)(size_t)(LAS char*)smem;
  const rsrc_t rA = mk_rsrc(Abase), rB = mk_rsrc(Bbase);
  unsigned voA[4];
  #pragma unroll
  for(int p=0;p<4;p++){ int row = (lw*4+p)*8 + (lane>>3); int c = (lane&7) ^ ((row>>1)&7);
    voA[p] = lin ? (unsigned)((lw*4+p)*1024 + lane*16) : (unsigned)(row*(SEQ*2) + c*16); }
  const unsigned astep = lin ? 16384u : 128u;
  const int bp0 = lw<2 ? lw*5 : 10 + (lw-2)*4;
  const unsigned voB = bp0*1024 + lane*16;
  constexpr int P = 4+NP_B;
  auto issue = [&](int st){
    int ks = st;
    if(lin){ const int g = st>=18 ? 1 : 0, q = st - 18*g, p = q/3; ks = p*6 + 3*g + (q - 3*p); }
    unsigned dst = lbase + (st&3)*K2_ST;
    #pragma unroll
    for(int p=0;p<4;p++) dma16c(rA, dst + (lw*4+p)*1024, voA[p], ks*astep);
    #pragma unroll
    for(int p=0;p<NP_B;p++) dma16c(rB, dst + K2_AB + (bp0+p)*1024, voB, ks*K2_BB + p*1024);
  };
  flag_wait(ctl, w0off, w0need, tag);
  issue(0); issue(1); issue(2);
  WAITV(2*P);
  BARRIER();
  for(int t=0;t<T;t++){
    if(t+2<T) WAITV(P); else WAITV(0);
    BARRIER();
    if(t==15) flag_wait(ctl, w1off, w1need, tag);
    if(t+3<T) issue(t+3);
  }
}

__global__ __launch_bounds__(512,2) void fused_kernel(const u16* __restrict__ x, const u16* __restrict__ cr, const u16* __restrict__ ci,
    const u16* __restrict__ wre, const u16* __restrict__ wim, const u16* __restrict__ w1, const u16* __restrict__ b1,
    const u16* __restrict__ w2, const u16* __restrict__ b2,
    char* himg, char* abimg, char* w2img, float* slab, char* ctl, u16* out, unsigned long long* stamps){
  extern __shared__ __attribute__((aligned(16))) char smem[];
  const int bid = blockIdx.x; const int xcd = bid&7, idx = bid>>3;
  const int wid = __builtin_amdgcn_readfirstlane(threadIdx.x>>6), lane = threadIdx.x&63;
  const int sp = (0x11330022 >> (4*xcd)) & 3;
  const int mt = (xcd&1)*8 + (idx>>2), nt = idx&3;
  const int tile = mt*4+nt;
  STAMP(0,0);
  const u16* cpf = (bid<128 ? cr : ci) + (size_t)((bid&127)*512 + wid*64)*64;
  if(idx>=30){
    if(wid==3) ctl_fetch(ctl, (unsigned)(size_t)(LAS char*)smem + K1_TAG, lane);
    touch_lines(cpf, (unsigned)(size_t)(LAS char*)smem + wid*256, lane);
    inbox_fill(slab, tile*4+sp, wid, lane);
    STAMP(0,11);
  } else {
  const int L = xcd*30+idx; const int mh = L/120, rem = L - mh*120; const int nb = rem>>3, mb = mh*8 + (rem&7);
  if(nb<12){ if(wid<4) k1_body<false,0>(smem,x,wre,wim,w1,b1,w2,himg,abimg,w2img,mb,nb,L,wid,lane,stamps,cpf,slab,ctl,tile*4+sp);
             else      k1_body<false,1>(smem,x,wre,wim,w1,b1,w2,himg,abimg,w2img,mb,nb,L,wid,lane,stamps,cpf,slab,ctl,tile*4+sp); }
  else     { if(wid<4) k1_body<true ,0>(smem,x,wre,wim,w1,b1,w2,himg,abimg,w2img,mb,nb,L,wid,lane,stamps,cpf,slab,ctl,tile*4+sp);
             else      k1_body<true ,1>(smem,x,wre,wim,w1,b1,w2,himg,abimg,w2img,mb,nb,L,wid,lane,stamps,cpf,slab,ctl,tile*4+sp); }
  }
  WAITV(0); WAITL0();
  BARRIER();
  const u64 tag = *(const u64*)(smem + K1_TAG) + 1;
  if(threadIdx.x==0) flag_set(ctl, 8192 + bid*8, tag);
  const int T = sp<2 ? 36 : 32;
  f32x4 acc[4][9];
  STAMP(1,0);
  if(wid>=4){
    const char* Abase; const char* Bbase;
    if(sp<2){ Abase = himg + (size_t)(mt*72 + sp*36)*16384; Bbase = w2img + (size_t)(nt*144 + sp*72)*9216; }
    else { Abase = (const char*)((sp==2?cr:ci) + (size_t)mt*128*SEQ); Bbase = abimg + (size_t)(((sp-2)*4+nt)*64)*9216; }
    unsigned w0off, w1off = 8192 + (idx*8 + ((0x4062 >> (4*(lane&3))) & 7) + (xcd&1))*8; bool w0need, w1need = (lane>>2)==2;
    if(sp<2){
      const int pw_ = wg_of(mt, 6*sp + (lane<6 ? lane : 0));
      const int Lw = 120*sp + 24*nt + (lane>=16 && lane<52 ? lane-16 : 0);
      w0need = lane<6 || (lane>=16 && lane<52);
      w0off = 4096 + (lane<16 ? pw_ : (Lw%30)*8 + Lw/30)*8;
      if(lane<6){ w1need = true; w1off = 8192 + pw_*8; }
    } else {
      const int ec = (sp-2)*576 + nt*144 + ((lane&16) ? 143 : 0);
      w0need = lane<32; w0off = 8192 + wg_of(lane&15, 12 + ec/384)*8;
    }
    if(wid<6) k2_loader<5>(smem, Abase, sp<2, Bbase, T, wid-4, lane, ctl, tag, w0off, w0need, w1off, w1need);
    else      k2_loader<4>(smem, Abase, sp<2, Bbase, T, wid-4, lane, ctl, tag, w0off, w0need, w1off, w1need);
  } else {
    const int mi = wid>>1, ki = wid&1;
    const int fr = lane&15, g = lane>>4;
    int aoff0, aoff1;
    if(sp<2){ aoff0 = 2*mi*4096 + (4*ki+g)*512 + fr*16; aoff1 = aoff0 + 256; }
    else { aoff0 = (64*mi + fr)*128 + (((4*ki+g) ^ (fr>>1))*16); aoff1 = aoff0 + 2048; }
    const int q = fr>>2, p = lane&3, ph = p>>1;
    const int boff0 = K2_AB + ki*9216 + ph*512 + (8*g + 4*ph + q)*16 + (p&1)*8;
    const int boff1 = K2_AB + ki*9216 + ph*512 + (8*g + 4*(1-ph) + q)*16 + (p&1)*8;
    #pragma unroll
    for(int m=0;m<4;m++)
      #pragma unroll
      for(int n=0;n<9;n++) acc[m][n] = (f32x4){0.f,0.f,0.f,0.f};
    constexpr int BD = K2BD;
    bf16x8 A0[4], A1[4], B[9];
    auto ldA = [&](bf16x8 (&A)[4], int st){ const char* p0 = smem + (st&3)*K2_ST;
      A[0] = *(const bf16x8*)(p0 + aoff0); A[1] = *(const bf16x8*)(p0 + aoff1);
      A[2] = *(const bf16x8*)(p0 + aoff0 + 4096); A[3] = *(const bf16x8*)(p0 + aoff1 + 4096); };
    auto ldB = [&](int st, int n)->bf16x8 { char* p0 = smem + (st&3)*K2_ST + n*1024;
      s16x4 lo = __builtin_amdgcn_ds_read_tr16_b64_v4i16((lds_s16x4)(p0 + boff0));
      s16x4 hi = __builtin_amdgcn_ds_read_tr16_b64_v4i16((lds_s16x4)(p0 + boff1));
      return __builtin_shufflevector(lo,hi,0,1,2,3,4,5,6,7); };
    BARRIER();
    STAMP(1,1);
    ldA(A0,0);
    #pragma unroll
    for(int n=0;n<BD;n++) B[n]=ldB(0,n);
    s16x4 Bl[9], Bh[9];
    #pragma unroll
    for(int n=0;n<BD;n++){ Bl[n] = __builtin_shufflevector(B[n],B[n],0,1,2,3); Bh[n] = __builtin_shufflevector(B[n],B[n],4,5,6,7); }
    const unsigned lb = (unsigned)(size_t)(LAS char*)smem;
    unsigned aA0[2] = {lb + aoff0, lb + aoff0 + 2*K2_ST}, aA1[2] = {lb + aoff1, lb + aoff1 + 2*K2_ST};
    unsigned aB0[2] = {lb + boff0, lb + boff0 + 2*K2_ST}, aB1[2] = {lb + boff1, lb + boff1 + 2*K2_ST};
    #pragma unroll
    for(int i=0;i<2;i++) asm volatile("" : "+v"(aA0[i]), "+v"(aA1[i]), "+v"(aB0[i]), "+v"(aB1[i]));
    auto step = [&](bf16x8 (&Ac)[4], bf16x8 (&An)[4], auto STC){
      constexpr int st = decltype(STC)::v, sn = (st+1)&3;
      #pragma unroll
      for(int n=0;n<9;n++){
        if(n+BD==9) BARRIER();
        const int np = n+BD<9 ? n+BD : n+BD-9;
        const int sb = n+BD<9 ? st : sn;
        const int ob = (sb&1)*K2_ST + np*1024;
        const bf16x8 Bc = __builtin_shufflevector(Bl[n],Bh[n],0,1,2,3,4,5,6,7);
        #pragma unroll
        for(int m=0;m<4;m++){
          acc[m][n] = __builtin_amdgcn_mfma_f32_16x16x32_bf16(Bc, Ac[m], acc[m][n], 0,0,0);
          SB();
          if(m==0) Bl[np] = __builtin_amdgcn_ds_read_tr16_b64_v4i16((lds_s16x4)((LAS char*)(aB0[sb>>1]) + ob));
          if(m==1) Bh[np] = __builtin_amdgcn_ds_read_tr16_b64_v4i16((lds_s16x4)((LAS char*)(aB1[sb>>1]) + ob));
          if(m==2 && n+BD>=9 && n+BD<=12){ const int a = n+BD-9;
            An[a] = *(const LAS bf16x8*)((LAS char*)((a&1) ? aA1[sn>>1] : aA0[sn>>1]) + (sn&1)*K2_ST + (a>>1)*4096); }
          SB();
        }
      }
    };
    for(int t=0;t<T;t+=4){ step(A0,A1,IC<0>{}); step(A1,A0,IC<1>{}); step(A0,A1,IC<2>{}); step(A1,A0,IC<3>{}); }
  }
  STAMP(1,2);
  BARRIER();
  if(wid<4){
    const int ki = wid&1;
    char* dst = smem + (wid^1)*18432 + lane*16;
    #pragma unroll
    for(int j=0;j<2;j++)
      #pragma unroll
      for(int n=0;n<9;n++){ f32x4 v = ki ? acc[j][n] : acc[2+j][n]; *(f32x4*)(dst + (j*9+n)*1024) = v; }
  }
  __syncthreads();
  STAMP(1,3);
  f32x4* own = (f32x4*)(smem + 4*18432);
  const rsrc_t rS = mk_rsrc(slab);
  if(wid<4){
    const int ki = wid&1;
    const char* src = smem + wid*18432 + lane*16;
    const unsigned so = (unsigned)(((tile*4 + wid)*4 + sp)*9216) + lane*16;
    #pragma unroll
    for(int n=0;n<9;n++){
      f32x4 v0 = (ki ? acc[2][n] : acc[0][n]) + *(const f32x4*)(src + n*1024);
      f32x4 v1 = (ki ? acc[3][n] : acc[1][n]) + *(const f32x4*)(src + (9+n)*1024);
      if(wid==sp){ own[n*64+lane] = v0; own[(9+n)*64+lane] = v1; }
      else { u32x4 w = {pk_bf16(v0[0],v0[1]), pk_bf16(v0[2],v0[3]), pk_bf16(v1[0],v1[1]), pk_bf16(v1[2],v1[3])};
        #pragma unroll
        for(int e=0;e<4;e++) w[e] = min(w[e], 0xfffffffeu);
        __builtin_amdgcn_raw_buffer_store_b128(w, rS, so + n*1024, 0, 16); }
    }
  }
  STAMP(1,4);
  const int fr = lane&15, g = lane>>4;
  const int nu = wid==4 ? 2 : 1;
  u32x2 xv[2][2], bv[2];
  #pragma unroll
  for(int i=0;i<2;i++){ if(i<nu){ const int n = i ? 8 : wid; const int col = nt*144 + 16*n + 4*g;
      #pragma unroll
      for(int j=0;j<2;j++) xv[i][j] = *(const u32x2*)(x + (size_t)(mt*128 + 32*sp + 16*j + fr)*DM + col);
      bv[i] = *(const u32x2*)(b2 + col); } }
  u32x4 pp[2][3];
  __syncthreads();
  { int spins = 0; unsigned done = 0;
    while(true){
      #pragma unroll
      for(int q=0;q<3;q++){ if(!((done>>q)&1)){ const int ss = q + (q>=sp ? 1 : 0);
        #pragma unroll
        for(int i=0;i<2;i++){ if(i<nu){ const int n = i ? 8 : wid;
          pp[i][q] = __builtin_amdgcn_raw_buffer_load_b128(rS, (unsigned)(((tile*4 + sp)*4 + ss)*9216) + n*1024 + lane*16, 0, 16); } } } }
      #pragma unroll
      for(int q=0;q<3;q++){ if(!((done>>q)&1)){ unsigned all = 0;
        #pragma unroll
        for(int i=0;i<2;i++){ if(i<nu) all = max(all, max(max(pp[i][q][0],pp[i][q][1]), max(pp[i][q][2],pp[i][q][3]))); }
        if(__all(all != 0xffffffffu)) done |= 1u<<q; } }
      if(done==7u) break;
      if(++spins > (1<<20)) break;
      __builtin_amdgcn_s_sleep(PSLEEP);
    } }
  STAMP(1,5);
  #pragma unroll
  for(int i=0;i<2;i++){ if(i<nu){ const int n = i ? 8 : wid; const int col = nt*144 + 16*n + 4*g;
    const float bf[4] = {__uint_as_float(bv[i].x<<16), __uint_as_float(bv[i].x&0xffff0000u), __uint_as_float(bv[i].y<<16), __uint_as_float(bv[i].y&0xffff0000u)};
    #pragma unroll
    for(int j=0;j<2;j++){
      const f32x4 ow = own[(j*9+n)*64+lane];
      f32x4 p[3];
      #pragma unroll
      for(int q=0;q<3;q++){ const unsigned a = pp[i][q][2*j], b = pp[i][q][2*j+1];
        p[q] = (f32x4){__uint_as_float(a<<16), __uint_as_float(a&0xffff0000u), __uint_as_float(b<<16), __uint_as_float(b&0xffff0000u)}; }
      f32x4 mm, sv;
      if(sp<2){ mm = ow + p[0]; sv = p[1] + p[2]; }
      else    { mm = p[0] + p[1]; sv = ow + p[2]; }
      const float xf[4] = {__uint_as_float(xv[i][j].x<<16), __uint_as_float(xv[i][j].x&0xffff0000u), __uint_as_float(xv[i][j].y<<16), __uint_as_float(xv[i][j].y&0xffff0000u)};
      float o[4];
      #pragma unroll
      for(int e=0;e<4;e++){ float m = mm[e]+bf[e];
        float gt = m * __builtin_amdgcn_rcpf(1.f + __builtin_amdgcn_exp2f(-1.44269504f*m));
        o[e] = xf[e] + sv[e]*gt; }
      *(u32x2*)(out + (size_t)(mt*128 + 32*sp + 16*j + fr)*DM + col) = (u32x2){pk_bf16(o[0],o[1]), pk_bf16(o[2],o[3])};
    } } }
  STAMP(1,6);
  if(bid==0 && wid==7){
    #pragma unroll
    for(int i=0;i<4;i++) flag_wait(ctl, 8192 + (i*64+lane)*8, true, tag);
    if(lane==0) flag_set(ctl, 0, tag);
  }
}

extern "C" void kernel_launch(void* const* d_in, const int* in_sizes, int n_in,
                              void* d_out, int out_size, void* d_ws, size_t ws_size,
                              hipStream_t stream) {
  const u16* x=(const u16*)d_in[0]; const u16* cr=(const u16*)d_in[1]; const u16* ci=(const u16*)d_in[2];
  const u16* wre=(const u16*)d_in[3]; const u16* wim=(const u16*)d_in[4];
  const u16* w1=(const u16*)d_in[6]; const u16* b1=(const u16*)d_in[7]; const u16* w2=(const u16*)d_in[8]; const u16* b2=(const u16*)d_in[9];
  char* ws=(char*)d_ws;
  char* himg = ws; char* abimg = ws + (20<<20); char* w2img = ws + (26<<20);
  float* slab=(float*)(ws+(32<<20)); unsigned* cnt=(unsigned*)(ws+(52<<20));
  unsigned long long* stamps = (unsigned long long*)(ws+(128<<20));
  (void)hipFuncSetAttribute((const void*)fused_kernel, hipFuncAttributeMaxDynamicSharedMemorySize, K1_LDS);
  fused_kernel<<<256,512,K1_LDS,stream>>>(x,cr,ci,wre,wim,w1,b1,w2,b2,himg,abimg,w2img,slab,(char*)cnt,(u16*)d_out,stamps);
}
```

```cpp
#include <hip/hip_runtime.h>
#include <hip/hip_bf16.h>
typedef unsigned short u16;
typedef short bf16x8 __attribute__((ext_vector_type(8)));
typedef short s16x4 __attribute__((ext_vector_type(4)));
typedef float f32x4 __attribute__((ext_vector_type(4)));
typedef float f32x2 __attribute__((ext_vector_type(2)));
typedef float f32x16 __attribute__((ext_vector_type(16)));
typedef unsigned u32x4 __attribute__((ext_vector_type(4)));
typedef unsigned u32x2 __attribute__((ext_vector_type(2)));
typedef __bf16 bf2 __attribute__((ext_vector_type(2)));
#define LAS __attribute__((address_space(3)))
typedef LAS s16x4* lds_s16x4;
typedef LAS void* lds_vp;
typedef __amdgpu_buffer_rsrc_t rsrc_t;
#define DI __device__ __forceinline__
DI unsigned pk_bf16(float lo, float hi){ bf2 v; v[0]=(__bf16)lo; v[1]=(__bf16)hi; return __builtin_bit_cast(unsigned,v); }
DI float opq(float x){ asm("" : "+v"(x)); return x; }
DI rsrc_t mk_rsrc(const void* p){ return __builtin_amdgcn_make_buffer_rsrc((void*)p, 0, 0x7fffffff, 0x00020000); }
constexpr int SEQ=2048, DM=576, FF=4608;

#define SB() __builtin_amdgcn_sched_barrier(0)
#define BARRIER() do{ SB(); asm volatile("":::"memory"); __builtin_amdgcn_s_barrier(); asm volatile("":::"memory"); SB(); }while(0)
#define WAITV(n) asm volatile("s_waitcnt vmcnt(%0)"::"n"(n):"memory")
#define WAITL0() asm volatile("s_waitcnt lgkmcnt(0)":::"memory")

DI void dma16(rsrc_t r, unsigned lds_addr, unsigned voff, unsigned soff){
  asm volatile("s_mov_b32 m0, %0\n\ts_nop 4\n\tbuffer_load_dwordx4 %1, %2, %3 offen lds"
               :: "s"(lds_addr), "v"(voff), "s"(r), "s"(soff) : "memory");
}

template<int N> struct IC { static constexpr int v = N; };
template<int B, int E, class Fn> DI void static_for(Fn&& f){ if constexpr (B<E){ f(IC<B>{}); static_for<B+1,E>(f); } }
constexpr int K1_U = 73728;
constexpr int K1_STAT = 2*K1_U;
constexpr int K1_B1   = K1_STAT + 2048;
constexpr int K1_CSP  = K1_B1 + 1536;
constexpr int K1_DUM  = K1_CSP + 2048;
constexpr int K1_TAG  = K1_DUM + 2048;
constexpr int K1_W2S  = K1_TAG + 256;
constexpr int K1_LDS  = K1_W2S + 8192;

template<int LOFF>
DI void dma16f(rsrc_t r, unsigned lds_base, unsigned voff, unsigned soff){
  asm volatile("s_add_u32 m0, %0, %1\n\ts_nop 3\n\tbuffer_load_dwordx4 %2, %3, %4 offen lds"
               :: "s"(lds_base), "i"(LOFF), "v"(voff), "s"(r), "s"(soff) : "memory", "scc");
}
typedef unsigned long long u64;
DI void inbox_fill(const void* slab, int tq, int wid, int lane){
  const u32x4 ff = {0xffffffffu,0xffffffffu,0xffffffffu,0xffffffffu};
  const rsrc_t rq = mk_rsrc(slab);
  #pragma unroll
  for(int i=0;i<4;i++){ const int p = wid + 8*i; if(p<27){ const int k3 = p/9, n = p - k3*9, src = k3 + (k3>=(tq&3) ? 1 : 0);
      __builtin_amdgcn_raw_buffer_store_b128(ff, rq, (unsigned)((tq*4 + src)*9216 + n*1024) + lane*16, 0, 16); } }
}
DI void ctl_fetch(const char* ctl, unsigned lds_dst, int lane){
  const rsrc_t rb = mk_rsrc(ctl);
  asm volatile("s_mov_b32 m0, %0\n\ts_nop 4\n\tbuffer_load_dword %1, %2, 0 offen sc1 lds" :: "s"(lds_dst), "v"(lane*4), "s"(rb) : "memory");
}
DI void flag_set(char* ctl, unsigned off, u64 tag){ __hip_atomic_store((u64*)(ctl + off), tag, __ATOMIC_RELAXED, __HIP_MEMORY_SCOPE_AGENT); }
DI void flag_wait(const char* ctl, unsigned off, bool need, u64 tag){
  const rsrc_t rb = mk_rsrc(ctl);
  for(int spins=0; spins<(1<<20); spins++){
    const u32x2 v = __builtin_amdgcn_raw_buffer_load_b64(rb, off, 0, 16);
    const bool ok = !need || (v[0]==(unsigned)tag && v[1]==(unsigned)(tag>>32));
    if(__all(ok)) break;
    __builtin_amdgcn_s_sleep(4);
  }
}
DI int wg_of(int mb, int nb){ const int L = (mb>>3)*120 + nb*8 + (mb&7); const int xc = L/30; return (L - xc*30)*8 + xc; }
DI void touch_lines(const u16* p, unsigned lds_dummy, int lane){
  const rsrc_t rb = mk_rsrc(p);
  asm volatile("s_mov_b32 m0, %0\n\ts_nop 4\n\tbuffer_load_dword %1, %2, 0 offen lds" :: "s"(lds_dummy), "v"(lane*128), "s"(rb) : "memory");
}
template<bool AB, int fb>
DI void k1_body(char* smem, const u16* x, const u16* wre, const u16* wim, const u16* w1, const u16* b1, const u16* w2,
                char* himg, char* abimg, char* w2img, int mb, int nb, int L, int wid, int lane, const u16* cpf, const void* slab, char* ctl, int ibx){
  const unsigned lbase = (unsigned)(size_t)(LAS char*)smem;
  const int w4 = wid&3;
  const int r = lane&31, hh = lane>>5;
  constexpr int WROW = AB ? 1152 : FF*2;
  constexpr int WKT  = AB ? 128 : 64*FF*2;
  const rsrc_t rX = mk_rsrc(x + (size_t)mb*128*DM);
  const unsigned voR0 = (lane>>3)*1152 + (((lane&7) ^ (lane>>4))*16);
  const unsigned voW0 = AB ? voR0 : (unsigned)((lane>>3)*(FF*2) + (((lane&7) ^ (((lane>>4)&1)<<2))*16));
  auto wrsrc = [&](int u)->rsrc_t {
    const int uc = u<=5 ? u : 5; const u16* p;
    if constexpr (AB){ int ec = (nb-12)*384 + uc*64; p = ec>=576 ? wim + (size_t)(ec-576)*DM : wre + (size_t)ec*DM; }
    else p = w1 + nb*384 + uc*64;
    return __builtin_amdgcn_make_buffer_rsrc((void*)p, 0, u<=5 ? 0x7fffffff : 0, 0x00020000);
  };
  if constexpr (!AB && fb==0){ if(wid<3){
    const rsrc_t rb = mk_rsrc(b1 + nb*384 + wid*128);
    asm volatile("s_mov_b32 m0, %0\n\ts_nop 4\n\tbuffer_load_dword %1, %2, 0 offen lds" :: "s"(lbase + K1_B1 + wid*256), "v"(lane*4), "s"(rb) : "memory"); } }
  if constexpr (fb==0){ if(wid==3) ctl_fetch(ctl, lbase + K1_TAG, lane); }
  const unsigned voXa = voR0 + wid*16*1152, voXb = (voR0^64) + (wid*16+8)*1152;
  const unsigned voS = (AB ? (voW0 ^ ((wid&1)<<6)) : voW0) + wid*8*WROW;
  const unsigned lbX = lbase + wid*2048, lbS = lbase + wid*1024;
  unsigned kto[9];
  { const int rho = (mb + 3*nb + nb/3) % 9;
    #pragma unroll
    for(int i=0;i<9;i++){ int t = i + rho; if(t>=9) t -= 9; kto[i] = t; } }
  #define DMAX(kt) do{ dma16f<((3*(kt))%18)*8192>(rX, lbX, voXa, kto[kt]*128); dma16f<((3*(kt))%18)*8192+1024>(rX, lbX, voXb, kto[kt]*128); }while(0)
  #define DMAW(rw,slot,kt) dma16f<(slot)*8192>(rw, lbS, voS, kto[kt]*WKT)
  const rsrc_t rw0 = wrsrc(0), rw1 = wrsrc(1), rw2 = wrsrc(2);
  DMAX(0); DMAW(rw0,2,0);
  DMAX(1); DMAW(rw0,5,1); DMAX(2); DMAW(rw0,8,2);

  unsigned xoff[4];
  unsigned woff[4];
  #pragma unroll
  for(int sp=0;sp<4;sp++){
    int s = sp ^ w4;
    int row = 32*w4 + r;
    xoff[sp] = lbase + row*128 + (((2*s+hh) ^ ((row>>1)&7))<<4);
    if constexpr (AB){ int wr = 32*fb + r; woff[sp] = wr*128 + (((2*s+hh) ^ ((wr>>1)&7))<<4); }
    else { int q=(lane&15)>>2, p=lane&3, blk=(lane>>4)&1; int wr = 16*s + 8*hh + q;
           woff[sp] = wr*128 + ((64*fb + 32*blk + 8*p) ^ (((q>>1)&1)<<6)); }
    woff[sp] += lbase;
  }
  auto wadv = [&](int d){
    #pragma unroll
    for(int sp=0;sp<4;sp++){ woff[sp] += d; asm volatile("" : "+v"(woff[sp])); } };
  auto ldW = [&](int off, int sp)->bf16x8 {
    LAS char* p = (LAS char*)(woff[sp]) + off;
    if constexpr (AB){ return *(const LAS bf16x8*)p; }
    else { s16x4 lo = __builtin_amdgcn_ds_read_tr16_b64_v4i16((lds_s16x4)p);
           s16x4 hi = __builtin_amdgcn_ds_read_tr16_b64_v4i16((lds_s16x4)(p + 512));
           return __builtin_shufflevector(lo,hi,0,1,2,3,4,5,6,7); }
  };
  const short selv = ((lane&15) == ((lane>>4)&1)) ? (short)0x3f80 : (short)0;
  const bf16x8 sel = {selv,selv,selv,selv,selv,selv,selv,selv};

  bf16x8 X[36];
  f32x16 P, Q;
  f32x4 cs = {0.f,0.f,0.f,0.f}, sx = {0.f,0.f,0.f,0.f};
  #pragma unroll
  for(int i=0;i<16;i++){ P[i]=0.f; Q[i]=0.f; }
  constexpr int PD = 3;
  bf16x8 pw[PD+1];
  if constexpr (fb==1) __builtin_amdgcn_s_setprio(1);
  bf16x8 wf[2][4];
  auto smma = [&](auto KT, auto RDC){
    constexpr int kt = decltype(KT)::v; constexpr bool RD = decltype(RDC)::v;
    #pragma unroll
    for(int sp=0;sp<4;sp++){
      P = __builtin_amdgcn_mfma_f32_32x32x16_bf16(wf[kt&1][sp], X[kt*4+sp], P, 0,0,0);
      if constexpr (RD){ SB(); X[(kt+1)*4+sp] = *(const LAS bf16x8*)((LAS char*)(xoff[sp]) + (((kt+1)&1) ? 24576 : 0)); wf[(kt+1)&1][sp] = ldW(16384,sp); SB(); }
      if(sp==0) cs = __builtin_amdgcn_mfma_f32_16x16x32_bf16(sel, wf[kt&1][0], cs, 0,0,0);
      if((sp>>1)==fb){ Q = __builtin_amdgcn_mfma_f32_32x32x16_bf16(X[kt*4+sp], X[kt*4+sp], Q, 0,0,0);
                       sx = __builtin_amdgcn_mfma_f32_16x16x32_bf16(sel, X[kt*4+sp], sx, 0,0,0); }
    }
    asm volatile("" : "+v"(Q));
    asm volatile("" : "+v"(P), "+v"(sx), "+v"(cs));
  };
  auto sstep = [&](auto KT){
    constexpr int kt = decltype(KT)::v;
    WAITV(6);
    WAITL0();
    BARRIER();
    auto lds = [&](){
      #pragma unroll
      for(int sp=0;sp<4;sp++){ X[kt*4+sp] = *(const LAS bf16x8*)((LAS char*)(xoff[sp]) + ((kt&1) ? 24576 : 0)); wf[kt&1][sp] = ldW(16384,sp); }
    };
    auto dmas = [&](){
      if constexpr (kt<=5){ DMAX(kt+3); DMAW(rw0,(3*(kt+3)+2)%18,kt+3); }
      else { DMAW(rw1,9+3*(kt-6),3*(kt-6)); DMAW(rw1,10+3*(kt-6),3*(kt-6)+1); DMAW(rw1,11+3*(kt-6),3*(kt-6)+2); }
    };
    if constexpr (kt==0){ lds(); SB(); dmas(); }
    else if constexpr (fb==0){ dmas(); SB(); lds(); SB(); smma(IC<kt-1>{}, IC<0>{}); }
    else     { smma(IC<kt-1>{}, IC<1>{}); SB(); dmas(); }
    wadv(kt==5 ? -5*24576 : 24576);
    if(kt&1){
      #pragma unroll
      for(int sp=0;sp<4;sp++){ xoff[sp] += (kt==5 ? -4*24576 : 2*24576); asm volatile("" : "+v"(xoff[sp])); } }
  };
  sstep(IC<0>{}); sstep(IC<1>{}); sstep(IC<2>{}); sstep(IC<3>{}); sstep(IC<4>{}); sstep(IC<5>{}); sstep(IC<6>{}); sstep(IC<7>{}); sstep(IC<8>{});
  smma(IC<8>{}, IC<0>{});
  if constexpr (fb==1) __builtin_amdgcn_s_setprio(0);
  auto put_cs = [&](int par){
    if(lane<16){ float* d = (float*)(smem + K1_CSP + par*1024 + wid*128 + lane*4); d[0] = cs[0]; d[16] = cs[1]; }
  };
  put_cs(0);
  if(lane<16){ float* d = (float*)(smem + K1_STAT + fb*512 + (32*w4+lane)*4); d[0] = sx[0]; d[16] = sx[1]; }
  { const int dr = (r&3) + 4*(r>>3); float dg = Q[0];
    #pragma unroll
    for(int i=1;i<16;i++) dg = (dr==i) ? Q[i] : dg;
    if(hh == ((r>>2)&1)) *(float*)(smem + K1_STAT + 1024 + fb*512 + (32*w4+r)*4) = dg; }
  WAITV(0); WAITL0();
  BARRIER();
  #pragma unroll
  for(int i=0;i<PD;i++) pw[i] = ldW(0,i);
  float rr, irr, K0, K1c; bf16x8 Bext;
  { const float* sp_ = (const float*)(smem + K1_STAT + (32*w4+r)*4); const float s1 = sp_[0] + sp_[128], s2 = sp_[256] + sp_[384];
    float mu = s1*(1.f/DM); float var = fmaxf(s2*(1.f/DM) - mu*mu, 0.f);
    float sd = __builtin_amdgcn_sqrtf(var + 1e-5f);
    irr = sd;
    if constexpr (!AB){ float v2 = var*__builtin_amdgcn_rcpf(var + 1e-5f); irr = sd*__builtin_amdgcn_sqrtf(v2 + 1e-5f); }
    rr = __builtin_amdgcn_rcpf(irr);
    K0 = -2.30220820f*rr; K1c = -0.10294324f*rr*rr*rr;
    unsigned i0 = pk_bf16(irr,irr) & 0xffff0000u; unsigned m0 = pk_bf16(-mu,-mu) & 0xffff0000u;
    unsigned wi = pk_bf16(irr, irr - __uint_as_float(i0)), wm = pk_bf16(-mu, -mu - __uint_as_float(m0));
    u32x4 bx = {wi, wm, wm, 0u};
    if(hh) bx = (u32x4){0u,0u,0u,0u};
    Bext = __builtin_bit_cast(bf16x8, bx); }
  const rsrc_t rH = mk_rsrc(AB ? abimg : himg);

  float cp4[4]; unsigned bw = 0; float to[2]; u32x2 pk[2];
  unsigned obase; float a_r = rr; int ce0=0, ri=0;
  auto epi_load = [&](int u){
    if constexpr (AB){ int ec = (nb-12)*384 + u*64; ri = ec>=576; ce0 = ((ec - ri*576)>>3) + 4*fb; a_r = ri ? -rr : rr; obase = hh*512 + ((r ^ (4*hh))*16); }
    else { obase = (unsigned)(((mb*72 + nb*6 + u)*4 + w4)*4096 + fb*2048) + lane*16;
           bw = *(const u16*)(smem + K1_B1 + u*128 + fb*64 + r*2); }
    const float* cp = (const float*)(smem + K1_CSP + (u&1)*1024 + fb*512);
    cp4[0] = cp[r]; cp4[1] = cp[32+r]; cp4[2] = cp[64+r]; cp4[3] = cp[96+r];
  };
  auto epi_fix = [&](f32x16& O){
    float c = opq(opq(cp4[0]+cp4[1]) + opq(cp4[2]+cp4[3]));
    unsigned c0 = pk_bf16(c,c); float c1 = c - __uint_as_float(c0 & 0xffff0000u);
    u32x4 ax = {bw*0x10001u, c0, pk_bf16(c1,c1), 0u};
    O = __builtin_amdgcn_mfma_f32_32x32x16_bf16(__builtin_bit_cast(bf16x8,ax), Bext, O, 0,0,0);
  };
  float tq[3]; u32x4 vst; unsigned ost;
  auto epi_q = [&](const f32x16& O, int e, int stg){
    float& t = tq[e%3];
    if constexpr (AB){
      if(stg==3) to[e&1] = opq(a_r*O[e]);
    } else {
      const float v = O[e];
      if(stg==0) t = opq(fmaf(opq(v*v), K1c, K0));
      if(stg==1) t = opq(__builtin_amdgcn_exp2f(opq(v*t)));
      if(stg==2) t = opq(__builtin_amdgcn_rcpf(opq(fmaf(t, irr, irr))));
      if(stg==3) to[e&1] = opq(v*t);
    }
    if(stg==3){
      if(e&1) pk[(e>>2)&1][(e>>1)&1] = pk_bf16(to[0],to[1]);
      if((e&7)==7){
        auto s0 = __builtin_amdgcn_permlane32_swap(pk[0][0], pk[1][0], false, false);
        auto s1 = __builtin_amdgcn_permlane32_swap(pk[0][1], pk[1][1], false, false);
        u32x4 v = {s0[0], s1[0], s0[1], s1[1]};
        unsigned so;
        if constexpr (AB){ int ce = ce0 + (e>>3)*2; int nt = ce/18, cin = ce - nt*18; so = (unsigned)((((ri*4+nt)*64 + mb*4 + w4)*18 + cin)*512); }
        else so = (e>>3)*1024;
        vst = v; ost = obase + so;
      }
    }
  };
  auto epi_store = [&](){ __builtin_amdgcn_raw_buffer_store_b128(vst, rH, ost, 0, 16); };
  auto epi_step = [&](const f32x16& O, int kk){
    if(kk<4) return;
    const int e1 = (kk-2)>>1, e0 = e1-1;
    if(kk<8) epi_q(O,0,kk-4);
    if(e0>=1) epi_q(O,e0,kk-2*e0-2);
    if(e1<=15) epi_q(O,e1,kk-2*e1-2);
  };
  rsrc_t rwa = rw2, rwb = wrsrc(3);
  auto interval = [&](f32x16& N, f32x16& O, int u, auto ODDC){
    constexpr int ODD = decltype(ODDC)::v & 1; constexpr bool FIRST = decltype(ODDC)::v == 3;
    epi_load(u-1);
    const int rli = (L>=120 ? L-120 : L) < 108 ? (L>=120 ? L-120 : L) : 107, rcb = rli/12, rkb = (L>=120 ? 72 : 0) + (rli - rcb*12)*6 + fb;
    if(!FIRST && u<=4){
      const u32x4 v = *(const LAS u32x4*)((LAS char*)(lbase + K1_W2S + fb*4096 + r*128 + (((2*w4+hh) ^ ((r>>1)&7))<<4)));
      const int ce = rcb*8 + 2*w4, nt_ = ce/18, cin = ce - nt_*18;
      __builtin_amdgcn_raw_buffer_store_b128(v, mk_rsrc(w2img), hh*512 + ((r ^ (4*hh))*16) + ((nt_*144 + rkb + 2*(u-2))*18 + cin)*512, 0, 16);
    }
    if(fb==0){ if(u==w4+1) touch_lines(cpf, lbase + K1_DUM + wid*256, lane); }
    static_for<0,36>([&](auto KKC){
      constexpr int kk = decltype(KKC)::v;
      constexpr int kp = kk+PD;
      if(kp==20) wadv(5*8192);
      if(kp==36) wadv(ODD ? -14*8192 : 4*8192);
      pw[kp%(PD+1)] = ldW(kp<20 ? (kp>>2)*8192 : (kp<36 ? ((kp-20)>>2)*8192 : 0), kp&3);
      if constexpr (FIRST){
        if(kk==0+fb) dma16f<0*8192>(rwa,lbS,voS,kto[0]*WKT);
        if(kk==2+fb) dma16f<1*8192>(rwa,lbS,voS,kto[1]*WKT);
        if(kk==4+fb) dma16f<2*8192>(rwa,lbS,voS,kto[2]*WKT);
        if(kk==6+fb) dma16f<3*8192>(rwa,lbS,voS,kto[3]*WKT);
        if(kk==8+fb) dma16f<4*8192>(rwa,lbS,voS,kto[4]*WKT);
        if(kk==10+fb) dma16f<5*8192>(rwa,lbS,voS,kto[5]*WKT);
        if(kk==12+fb) dma16f<6*8192>(rwa,lbS,voS,kto[6]*WKT);
        if(kk==14+fb) dma16f<7*8192>(rwa,lbS,voS,kto[7]*WKT);
        if(kk==16+fb) dma16f<8*8192>(rwa,lbS,voS,kto[8]*WKT);
      } else {
      if(kk==1+2*fb) dma16f<(1-ODD)*K1_U+5*8192>(rwa,lbS,voS,kto[5]*WKT);
      if(kk==5+2*fb) dma16f<(1-ODD)*K1_U+6*8192>(rwa,lbS,voS,kto[6]*WKT);
      if(kk==9+2*fb) dma16f<(1-ODD)*K1_U+7*8192>(rwa,lbS,voS,kto[7]*WKT);
      if(kk==13+2*fb) dma16f<(1-ODD)*K1_U+8*8192>(rwa,lbS,voS,kto[8]*WKT);
      }
      if(kk==21+fb ) dma16f<ODD*K1_U+0*8192>(rwb,lbS,voS,kto[0]*WKT);
      if(kk==24+fb ) dma16f<ODD*K1_U+1*8192>(rwb,lbS,voS,kto[1]*WKT);
      if(kk==27+fb ) dma16f<ODD*K1_U+2*8192>(rwb,lbS,voS,kto[2]*WKT);
      if(kk==30+fb ) dma16f<ODD*K1_U+3*8192>(rwb,lbS,voS,kto[3]*WKT);
      if(kk==32+fb ) dma16f<ODD*K1_U+4*8192>(rwb,lbS,voS,kto[4]*WKT);
      SB();
      if(kk==0){ f32x16 z;
        #pragma unroll
        for(int i=0;i<16;i++) z[i]=0.f;
        N = __builtin_amdgcn_mfma_f32_32x32x16_bf16(pw[0], X[0], z, 0,0,0);
        cs = __builtin_amdgcn_mfma_f32_16x16x32_bf16(sel, pw[0], (f32x4){0.f,0.f,0.f,0.f}, 0,0,0); }
      else { N = __builtin_amdgcn_mfma_f32_32x32x16_bf16(pw[kk%(PD+1)], X[kk], N, 0,0,0);
        if((kk&3)==0) cs = __builtin_amdgcn_mfma_f32_16x16x32_bf16(sel, pw[kk%(PD+1)], cs, 0,0,0);
      }
      if(kk==2) epi_fix(O);
      epi_step(O, kk);
      if(kk==20) epi_store();
      if(kk==0 && !FIRST) epi_store();
      if(kk==34){ put_cs(ODD); rwa = rwb; rwb = wrsrc(u+3); }
      SB();
      if(kk==19){ if constexpr (FIRST) WAITV(8); else WAITV(4); BARRIER();
        if(u<=3) dma16(mk_rsrc(w2), lbase + K1_W2S + wid*1024, ((lane>>3)*1152 + (((lane&7) ^ (lane>>4) ^ ((w4&1)<<2))*16)), (unsigned)(((rkb + 2*(u-1))*32 + w4*8)*1152 + rcb*128));
        if(fb==1){ if(u==(w4<3 ? w4+1 : 3)) touch_lines(cpf, lbase + K1_DUM + wid*256, lane); } }
      if(kk==34 && !ODD){ if(u==4) WAITV(0); }
      if(kk==35){ WAITV(5); asm volatile("s_waitcnt lgkmcnt(%0)"::"n"(AB ? 1 : 2):"memory"); BARRIER();
        if constexpr (!ODD && fb==0){ if(u==4 && wid==0){ const u64 tag = *(const u64*)(smem + K1_TAG) + 1; if(lane==0) flag_set(ctl, 4096 + blockIdx.x*8, tag); } } }
    });
  };
  interval(Q, P, 1, IC<3>{});
  #pragma unroll 1
  for(int it=1; it<3; it++){
    interval(P, Q, 2*it, IC<0>{});
    interval(Q, P, 2*it+1, IC<1>{});
  }
  epi_store();
  inbox_fill(slab, ibx, wid, lane);
  epi_load(5);
  epi_fix(Q);
  static_for<4,36>([&](auto KKC){ epi_step(Q,decltype(KKC)::v); if(decltype(KKC)::v==19 || decltype(KKC)::v==35) epi_store(); });
}

constexpr int K2_AB = 128*128;
constexpr int K2_BB = 64*288;
constexpr int K2_ST = K2_AB + K2_BB;
constexpr int K2_LDS = K2_ST*4;

DI void dma16c(rsrc_t r, unsigned lds_addr, unsigned voff, unsigned soff){
  asm volatile("s_mov_b32 m0, %0\n\ts_nop 4\n\tbuffer_load_dwordx4 %1, %2, %3 offen sc1 lds"
               :: "s"(lds_addr), "v"(voff), "s"(r), "s"(soff) : "memory");
}
template<int NP_B>
DI void k2_loader(char* smem, const char* Abase, bool lin, const char* Bbase, int T, int lw, int lane,
                  const char* ctl, u64 tag, unsigned w0off, bool w0need, unsigned w1off, bool w1need, bool keeper){
  const unsigned lbase = (unsigned)(size_t)(LAS char*)smem;
  const rsrc_t rA = mk_rsrc(Abase), rB = mk_rsrc(Bbase);
  unsigned voA[4];
  #pragma unroll
  for(int p=0;p<4;p++){ int row = (lw*4+p)*8 + (lane>>3); int c = (lane&7) ^ ((row>>1)&7);
    voA[p] = lin ? (unsigned)((lw*4+p)*1024 + lane*16) : (unsigned)(row*(SEQ*2) + c*16); }
  const unsigned astep = lin ? 16384u : 128u;
  const int bp0 = lw<2 ? lw*5 : 10 + (lw-2)*4;
  const unsigned voB = bp0*1024 + lane*16;
  constexpr int P = 4+NP_B;
  auto issue = [&](int st){
    int ks = st;
    if(lin){ const int g = st>=18 ? 1 : 0, q = st - 18*g, p = q/3; ks = p*6 + 3*g + (q - 3*p); }
    unsigned dst = lbase + (st&3)*K2_ST;
    #pragma unroll
    for(int p=0;p<4;p++) dma16c(rA, dst + (lw*4+p)*1024, voA[p], ks*astep);
    #pragma unroll
    for(int p=0;p<NP_B;p++) dma16c(rB, dst + K2_AB + (bp0+p)*1024, voB, ks*K2_BB + p*1024);
  };
  flag_wait(ctl, w0off, w0need, tag);
  issue(0); issue(1); issue(2);
  WAITV(2*P);
  BARRIER();
  for(int t=0;t<T;t++){
    if(t+2<T) WAITV(P); else WAITV(0);
    BARRIER();
    if(t==15) flag_wait(ctl, w1off, w1need, tag);
    if(keeper && t==22){
      const rsrc_t rb = mk_rsrc(ctl); const unsigned lo = (unsigned)tag, hi = (unsigned)(tag>>32);
      for(int spins=0; spins<(1<<20); spins++){
        const u32x4 a = __builtin_amdgcn_raw_buffer_load_b128(rb, 8192 + lane*16, 0, 16), b = __builtin_amdgcn_raw_buffer_load_b128(rb, 9216 + lane*16, 0, 16);
        if(__all(a[0]==lo && a[1]==hi && a[2]==lo && a[3]==hi && b[0]==lo && b[1]==hi && b[2]==lo && b[3]==hi)) break;
        __builtin_amdgcn_s_sleep(4); }
      if(lane==0) flag_set((char*)ctl, 0, tag); }
    if(t+3<T) issue(t+3);
  }
}

__global__ __launch_bounds__(512,2) void fused_kernel(const u16* __restrict__ x, const u16* __restrict__ cr, const u16* __restrict__ ci,
    const u16* __restrict__ wre, const u16* __restrict__ wim, const u16* __restrict__ w1, const u16* __restrict__ b1,
    const u16* __restrict__ w2, const u16* __restrict__ b2,
    char* himg, char* abimg, char* w2img, float* slab, char* ctl, u16* out){
  extern __shared__ __attribute__((aligned(16))) char smem[];
  const int bid = blockIdx.x; const int xcd = bid&7, idx = bid>>3;
  const int wid = __builtin_amdgcn_readfirstlane(threadIdx.x>>6), lane = threadIdx.x&63;
  const int sp = (0x11330022 >> (4*xcd)) & 3;
  const int mt = (xcd&1)*8 + (idx>>2), nt = idx&3;
  const int tile = mt*4+nt;
  const u16* cpf = (bid<128 ? cr : ci) + (size_t)((bid&127)*512 + wid*64)*64;
  if(idx>=30){
    if(wid==3) ctl_fetch(ctl, (unsigned)(size_t)(LAS char*)smem + K1_TAG, lane);
    touch_lines(cpf, (unsigned)(size_t)(LAS char*)smem + wid*256, lane);
    inbox_fill(slab, tile*4+sp, wid, lane);
  } else {
  const int L = xcd*30+idx; const int mh = L/120, rem = L - mh*120; const int nb = rem>>3, mb = mh*8 + (rem&7);
  if(nb<12){ if(wid<4) k1_body<false,0>(smem,x,wre,wim,w1,b1,w2,himg,abimg,w2img,mb,nb,L,wid,lane,cpf,slab,ctl,tile*4+sp);
             else      k1_body<false,1>(smem,x,wre,wim,w1,b1,w2,himg,abimg,w2img,mb,nb,L,wid,lane,cpf,slab,ctl,tile*4+sp); }
  else     { if(wid<4) k1_body<true ,0>(smem,x,wre,wim,w1,b1,w2,himg,abimg,w2img,mb,nb,L,wid,lane,cpf,slab,ctl,tile*4+sp);
             else      k1_body<true ,1>(smem,x,wre,wim,w1,b1,w2,himg,abimg,w2img,mb,nb,L,wid,lane,cpf,slab,ctl,tile*4+sp); }
  }
  WAITV(0); WAITL0();
  BARRIER();
  const u64 tag = *(const u64*)(smem + K1_TAG) + 1;
  if(threadIdx.x==0) flag_set(ctl, 8192 + bid*8, tag);
  const int T = sp<2 ? 36 : 32;
  f32x4 acc[4][9];
  if(wid>=4){
    const char* Abase; const char* Bbase;
    if(sp<2){ Abase = himg + (size_t)(mt*72 + sp*36)*16384; Bbase = w2img + (size_t)(nt*144 + sp*72)*9216; }
    else { Abase = (const char*)((sp==2?cr:ci) + (size_t)mt*128*SEQ); Bbase = abimg + (size_t)(((sp-2)*4+nt)*64)*9216; }
    unsigned w0off, w1off = 8192 + (idx*8 + ((0x4062 >> (4*(lane&3))) & 7) + (xcd&1))*8; bool w0need, w1need = (lane>>2)==2;
    if(sp<2){
      const int pw_ = wg_of(mt, 6*sp + (lane<6 ? lane : 0));
      const int Lw = 120*sp + 24*nt + (lane>=16 && lane<52 ? lane-16 : 0);
      w0need = lane<6 || (lane>=16 && lane<52);
      w0off = 4096 + (lane<16 ? pw_ : (Lw%30)*8 + Lw/30)*8;
      if(lane<6){ w1need = true; w1off = 8192 + pw_*8; }
    } else {
      const int ec = (sp-2)*576 + nt*144 + ((lane&16) ? 143 : 0);
      w0need = lane<32; w0off = 8192 + wg_of(lane&15, 12 + ec/384)*8;
    }
    if(wid<6) k2_loader<5>(smem, Abase, sp<2, Bbase, T, wid-4, lane, ctl, tag, w0off, w0need, w1off, w1need, false);
    else      k2_loader<4>(smem, Abase, sp<2, Bbase, T, wid-4, lane, ctl, tag, w0off, w0need, w1off, w1need, bid==0 && wid==7);
  } else {
    const int mi = wid>>1, ki = wid&1;
    const int fr = lane&15, g = lane>>4;
    int aoff0, aoff1;
    if(sp<2){ aoff0 = 2*mi*4096 + (4*ki+g)*512 + fr*16; aoff1 = aoff0 + 256; }
    else { aoff0 = (64*mi + fr)*128 + (((4*ki+g) ^ (fr>>1))*16); aoff1 = aoff0 + 2048; }
    const int q = fr>>2, p = lane&3, ph = p>>1;
    const int boff0 = K2_AB + ki*9216 + ph*512 + (8*g + 4*ph + q)*16 + (p&1)*8;
    const int boff1 = K2_AB + ki*9216 + ph*512 + (8*g + 4*(1-ph) + q)*16 + (p&1)*8;
    #pragma unroll
    for(int m=0;m<4;m++)
      #pragma unroll
      for(int n=0;n<9;n++) acc[m][n] = (f32x4){0.f,0.f,0.f,0.f};
    constexpr int BD = 5;
    bf16x8 A0[4], A1[4], B[9];
    auto ldA = [&](bf16x8 (&A)[4], int st){ const char* p0 = smem + (st&3)*K2_ST;
      A[0] = *(const bf16x8*)(p0 + aoff0); A[1] = *(const bf16x8*)(p0 + aoff1);
      A[2] = *(const bf16x8*)(p0 + aoff0 + 4096); A[3] = *(const bf16x8*)(p0 + aoff1 + 4096); };
    auto ldB = [&](int st, int n)->bf16x8 { char* p0 = smem + (st&3)*K2_ST + n*1024;
      s16x4 lo = __builtin_amdgcn_ds_read_tr16_b64_v4i16((lds_s16x4)(p0 + boff0));
      s16x4 hi = __builtin_amdgcn_ds_read_tr16_b64_v4i16((lds_s16x4)(p0 + boff1));
      return __builtin_shufflevector(lo,hi,0,1,2,3,4,5,6,7); };
    BARRIER();
    ldA(A0,0);
    #pragma unroll
    for(int n=0;n<BD;n++) B[n]=ldB(0,n);
    s16x4 Bl[9], Bh[9];
    #pragma unroll
    for(int n=0;n<BD;n++){ Bl[n] = __builtin_shufflevector(B[n],B[n],0,1,2,3); Bh[n] = __builtin_shufflevector(B[n],B[n],4,5,6,7); }
    const unsigned lb = (unsigned)(size_t)(LAS char*)smem;
    unsigned aA0[2] = {lb + aoff0, lb + aoff0 + 2*K2_ST}, aA1[2] = {lb + aoff1, lb + aoff1 + 2*K2_ST};
    unsigned aB0[2] = {lb + boff0, lb + boff0 + 2*K2_ST}, aB1[2] = {lb + boff1, lb + boff1 + 2*K2_ST};
    #pragma unroll
    for(int i=0;i<2;i++) asm volatile("" : "+v"(aA0[i]), "+v"(aA1[i]), "+v"(aB0[i]), "+v"(aB1[i]));
    auto step = [&](bf16x8 (&Ac)[4], bf16x8 (&An)[4], auto STC){
      constexpr int st = decltype(STC)::v, sn = (st+1)&3;
      #pragma unroll
      for(int n=0;n<9;n++){
        if(n+BD==9) BARRIER();
        const int np = n+BD<9 ? n+BD : n+BD-9;
        const int sb = n+BD<9 ? st : sn;
        const int ob = (sb&1)*K2_ST + np*1024;
        const bf16x8 Bc = __builtin_shufflevector(Bl[n],Bh[n],0,1,2,3,4,5,6,7);
        #pragma unroll
        for(int m=0;m<4;m++){
          acc[m][n] = __builtin_amdgcn_mfma_f32_16x16x32_bf16(Bc, Ac[m], acc[m][n], 0,0,0);
          SB();
          if(m==0) Bl[np] = __builtin_amdgcn_ds_read_tr16_b64_v4i16((lds_s16x4)((LAS char*)(aB0[sb>>1]) + ob));
          if(m==1) Bh[np] = __builtin_amdgcn_ds_read_tr16_b64_v4i16((lds_s16x4)((LAS char*)(aB1[sb>>1]) + ob));
          if(m==2 && n+BD>=9 && n+BD<=12){ const int a = n+BD-9;
            An[a] = *(const LAS bf16x8*)((LAS char*)((a&1) ? aA1[sn>>1] : aA0[sn>>1]) + (sn&1)*K2_ST + (a>>1)*4096); }
          SB();
        }
      }
    };
    for(int t=0;t<T;t+=4){ step(A0,A1,IC<0>{}); step(A1,A0,IC<1>{}); step(A0,A1,IC<2>{}); step(A1,A0,IC<3>{}); }
  }
  BARRIER();
  if(wid<4){
    const int ki = wid&1;
    char* dst = smem + (wid^1)*18432 + lane*16;
    #pragma unroll
    for(int j=0;j<2;j++)
      #pragma unroll
      for(int n=0;n<9;n++){ f32x4 v = ki ? acc[j][n] : acc[2+j][n]; *(f32x4*)(dst + (j*9+n)*1024) = v; }
  }
  __syncthreads();
  f32x4* own = (f32x4*)(smem + 4*18432);
  const rsrc_t rS = mk_rsrc(slab);
  if(wid<4){
    const int ki = wid&1;
    const char* src = smem + wid*18432 + lane*16;
    const unsigned so = (unsigned)(((tile*4 + wid)*4 + sp)*9216) + lane*16;
    #pragma unroll
    for(int n=0;n<9;n++){
      f32x4 v0 = (ki ? acc[2][n] : acc[0][n]) + *(const f32x4*)(src + n*1024);
      f32x4 v1 = (ki ? acc[3][n] : acc[1][n]) + *(const f32x4*)(src + (9+n)*1024);
      if(wid==sp){ own[n*64+lane] = v0; own[(9+n)*64+lane] = v1; }
      else { u32x4 w = {pk_bf16(v0[0],v0[1]), pk_bf16(v0[2],v0[3]), pk_bf16(v1[0],v1[1]), pk_bf16(v1[2],v1[3])};
        #pragma unroll
        for(int e=0;e<4;e++) w[e] = min(w[e], 0xfffffffeu);
        __builtin_amdgcn_raw_buffer_store_b128(w, rS, so + n*1024, 0, 16); }
    }
  }
  const int fr = lane&15, g = lane>>4;
  const int nu = wid==4 ? 2 : 1;
  u32x2 xv[2][2], bv[2];
  #pragma unroll
  for(int i=0;i<2;i++){ if(i<nu){ const int n = i ? 8 : wid; const int col = nt*144 + 16*n + 4*g;
      #pragma unroll
      for(int j=0;j<2;j++) xv[i][j] = *(const u32x2*)(x + (size_t)(mt*128 + 32*sp + 16*j + fr)*DM + col);
      bv[i] = *(const u32x2*)(b2 + col); } }
  u32x4 pp[2][3];
  __syncthreads();
  { int spins = 0; unsigned done = 0;
    while(true){
      #pragma unroll
      for(int q=0;q<3;q++){ if(!((done>>q)&1)){ const int ss = q + (q>=sp ? 1 : 0);
        #pragma unroll
        for(int i=0;i<2;i++){ if(i<nu){ const int n = i ? 8 : wid;
          pp[i][q] = __builtin_amdgcn_raw_buffer_load_b128(rS, (unsigned)(((tile*4 + sp)*4 + ss)*9216) + n*1024 + lane*16, 0, 16); } } } }
      #pragma unroll
      for(int q=0;q<3;q++){ if(!((done>>q)&1)){ unsigned all = 0;
        #pragma unroll
        for(int i=0;i<2;i++){ if(i<nu) all = max(all, max(max(pp[i][q][0],pp[i][q][1]), max(pp[i][q][2],pp[i][q][3]))); }
        if(__all(all != 0xffffffffu)) done |= 1u<<q; } }
      if(done==7u) break;
      if(++spins > (1<<20)) break;
      __builtin_amdgcn_s_sleep(2);
    } }
  #pragma unroll
  for(int i=0;i<2;i++){ if(i<nu){ const int n = i ? 8 : wid; const int col = nt*144 + 16*n + 4*g;
    const float bf[4] = {__uint_as_float(bv[i].x<<16), __uint_as_float(bv[i].x&0xffff0000u), __uint_as_float(bv[i].y<<16), __uint_as_float(bv[i].y&0xffff0000u)};
    #pragma unroll
    for(int j=0;j<2;j++){
      const f32x4 ow = own[(j*9+n)*64+lane];
      f32x4 p[3];
      #pragma unroll
      for(int q=0;q<3;q++){ const unsigned a = pp[i][q][2*j], b = pp[i][q][2*j+1];
        p[q] = (f32x4){__uint_as_float(a<<16), __uint_as_float(a&0xffff0000u), __uint_as_float(b<<16), __uint_as_float(b&0xffff0000u)}; }
      f32x4 mm, sv;
      if(sp<2){ mm = ow + p[0]; sv = p[1] + p[2]; }
      else    { mm = p[0] + p[1]; sv = ow + p[2]; }
      const float xf[4] = {__uint_as_float(xv[i][j].x<<16), __uint_as_float(xv[i][j].x&0xffff0000u), __uint_as_float(xv[i][j].y<<16), __uint_as_float(xv[i][j].y&0xffff0000u)};
      float o[4];
      #pragma unroll
      for(int e=0;e<4;e++){ float m = mm[e]+bf[e];
        float gt = m * __builtin_amdgcn_rcpf(1.f + __builtin_amdgcn_exp2f(-1.44269504f*m));
        o[e] = xf[e] + sv[e]*gt; }
      *(u32x2*)(out + (size_t)(mt*128 + 32*sp + 16*j + fr)*DM + col) = (u32x2){pk_bf16(o[0],o[1]), pk_bf16(o[2],o[3])};
    } } }
}

extern "C" void kernel_launch(void* const* d_in, const int* in_sizes, int n_in,
                              void* d_out, int out_size, void* d_ws, size_t ws_size,
                              hipStream_t stream) {
  const u16* x=(const u16*)d_in[0]; const u16* cr=(const u16*)d_in[1]; const u16* ci=(const u16*)d_in[2];
  const u16* wre=(const u16*)d_in[3]; const u16* wim=(const u16*)d_in[4];
  const u16* w1=(const u16*)d_in[6]; const u16* b1=(const u16*)d_in[7]; const u16* w2=(const u16*)d_in[8]; const u16* b2=(const u16*)d_in[9];
  char* ws=(char*)d_ws;
  char* himg = ws; char* abimg = ws + (20<<20); char* w2img = ws + (26<<20);
  float* slab=(float*)(ws+(32<<20)); unsigned* cnt=(unsigned*)(ws+(52<<20));
  (void)hipFuncSetAttribute((const void*)fused_kernel, hipFuncAttributeMaxDynamicSharedMemorySize, K1_LDS);
  fused_kernel<<<256,512,K1_LDS,stream>>>(x,cr,ci,wre,wim,w1,b1,w2,b2,himg,abimg,w2img,slab,(char*)cnt,(u16*)d_out);
}
```
